```python
import torch

def _softmax(x, axis=-1):
    return torch.softmax(x, dim=axis)
def _gelu(x):
    return torch.nn.functional.gelu(x, approximate="tanh")
def _silu(x):
    return torch.nn.functional.silu(x)
def _sigmoid(x):
    return torch.sigmoid(x)
def _layernorm(x, axis=-1, eps=1e-5):
    m = x.mean(dim=axis, keepdim=True)
    v = x.var(dim=axis, keepdim=True, unbiased=False)
    return (x - m) / torch.sqrt(v + eps)
def _scatter_add(tgt, idx, src, axis=0):
    return tgt.clone().index_add_(axis, idx, src)
def _segment_sum(src, seg, n):
    out = torch.zeros((n,) + src.shape[1:], dtype=src.dtype)
    return out.index_add_(0, seg, src)
def _pad(x, pw):
    flat = tuple(v for pair in reversed(pw) for v in pair)
    return torch.nn.functional.pad(x, flat)

kernel_name = 'gated_mix__gqa_block_cached__dense_activation_gelu__decode'

def setup_inputs():

    _w = (lambda a: (lambda t: t.to(torch.bfloat16) if t.is_floating_point() else t)(torch.as_tensor(a)))
    def _A_setup():
        B, Sq, D = 1, 1, 448
        Hq, Hkv, Sk, Dh = 16, 8, 65536, 256
        assert Hq % Hkv == 0, (Hq, Hkv)
        return {
            "x": _w((lambda *s: torch.randn(*s, dtype=torch.bfloat16) * 0.05)(B, Sq, D)),
            "k_cache": _w((lambda *s: torch.randn(*s, dtype=torch.bfloat16) * 6.4)(B, Hkv, Sk, Dh)),
            "v_cache": _w((lambda *s: torch.randn(*s, dtype=torch.bfloat16) * 0.4)(B, Hkv, Sk, Dh)),
            "wq": _w((lambda *s: torch.randn(*s, dtype=torch.bfloat16) * 6.4)(D, Hq * Dh) * (D ** -0.5)),
            "wo": _w((lambda *s: torch.randn(*s, dtype=torch.bfloat16) * 0.4)(Hq * Dh, D) * ((Hq * Dh) ** -0.5)),
        }
    _A_in = _A_setup()

    def _B_setup():
        B, S, D, F = 1, 1, 448, 1792
        return {
            "x": _w((lambda *s: torch.randn(*s, dtype=torch.bfloat16) * 0.05)(B, S, D)),
            "w1": _w((lambda *s: torch.randn(*s, dtype=torch.bfloat16) * 3.2)(D, F) * (D ** -0.5)),
            "b1": _w((lambda *s: torch.randn(*s, dtype=torch.bfloat16) * 3.2)(F) * 0.01),
            "w2": _w((lambda *s: torch.randn(*s, dtype=torch.bfloat16) * 0.2)(F, D) * (F ** -0.5)),
            "b2": _w((lambda *s: torch.randn(*s, dtype=torch.bfloat16) * 0.4)(D) * 0.01),
        }
    _B_in = _B_setup()

    return {
        "A_x": _A_in["x"],
        "A_k_cache": _A_in["k_cache"],
        "A_v_cache": _A_in["v_cache"],
        "A_wq": _A_in["wq"],
        "A_wo": _A_in["wo"],
        "B_x": _B_in["x"],
        "B_w1": _B_in["w1"],
        "B_b1": _B_in["b1"],
        "B_w2": _B_in["w2"],
        "B_b2": _B_in["b2"],
        "gate_w": _w((lambda *s: torch.randn(*s, dtype=torch.bfloat16) * 0.05)(448) * 16.0),
    }

def reference(A_x, A_k_cache, A_v_cache, A_wq, A_wo, B_x, B_w1, B_b1, B_w2, B_b2, gate_w):
    def _A_ref(x, k_cache, v_cache, wq, wo):
        B, Sq, D = x.shape
        Hq, Dh = 16, 256
        Hkv = k_cache.shape[1]
        rep = Hq // Hkv
        q = (lambda x, *d: x.permute(*d))((x @ wq).reshape(B, Sq, Hq, Dh), 0, 2, 1, 3)
        k = (lambda x, n, axis: torch.repeat_interleave(x, n, dim=axis))(k_cache, rep, axis=1)
        v = (lambda x, n, axis: torch.repeat_interleave(x, n, dim=axis))(v_cache, rep, axis=1)
        scores = torch.einsum("bhqd,bhkd->bhqk", q, k) * (Dh ** -0.5)
        p = _softmax(scores, axis=-1)
        o = torch.einsum("bhqk,bhkd->bhqd", p, v)
        return (lambda x, *d: x.permute(*d))(o, 0, 2, 1, 3).reshape(B, Sq, Hq * Dh) @ wo

    def _B_ref(x, w1, b1, w2, b2):
        h = torch.einsum("bsd,df->bsf", x, w1) + b1
        h = _gelu(h)
        return torch.einsum("bsf,fd->bsd", h, w2) + b2

    x = A_x
    g = _sigmoid((x * gate_w).sum(-1, keepdims=True))
    a = _A_ref(x, A_k_cache, A_v_cache, A_wq, A_wo)
    b = _B_ref(x, B_w1, B_b1, B_w2, B_b2)
    return g * a + (1 - g) * b
```

```cpp
#include <hip/hip_runtime.h>
#include <hip/hip_bf16.h>
#include <stdint.h>

typedef unsigned u32x4 __attribute__((ext_vector_type(4)));
typedef unsigned u32x2 __attribute__((ext_vector_type(2)));
typedef float f32x4 __attribute__((ext_vector_type(4)));
typedef __bf16 bf16x2_t __attribute__((ext_vector_type(2)));
typedef unsigned long long u64;

#define STAMPS 0

namespace {
constexpr int DM = 448, HKV = 8, SK = 65536, DH = 256, FF = 1792, QW = 4096;
constexpr int NWG = 256, NSW = 4, NTHR = (NSW + 1) * 64;
constexpr int T = 16;
constexpr int GW = 32 * NSW;
constexpr int NTILE = SK / (2 * T);
constexpr int NSTEP = NTILE / GW;
constexpr unsigned HEAD_BYTES = (unsigned)SK * DH * 2;
constexpr int NRED = 112;
constexpr unsigned SENT = 0xFFFFFFFFu;
constexpr float C2 = 0.0625f * 1.4426950408889634f;
constexpr unsigned WS_EPOCH = 0;
constexpr unsigned WS_Q = 4096;
constexpr unsigned WS_S1 = WS_Q + 8 * 512 * 8;
constexpr unsigned WS_P1 = WS_S1 + 8 * 2 * 32 * 8;
constexpr unsigned WS_P2 = WS_P1 + 8 * 32 * 32 * 64;
constexpr unsigned WS_END = WS_P2 + NRED * NWG * 16;
constexpr unsigned WS_STAMP = 4u << 20;
constexpr unsigned SPIN_CAP = 1u << 18;

__device__ __forceinline__ float dot2(unsigned a, unsigned b, float c) {
  return __builtin_amdgcn_fdot2_f32_bf16(__builtin_bit_cast(bf16x2_t, a), __builtin_bit_cast(bf16x2_t, b), c, false);
}
template <int CTRL> __device__ __forceinline__ float dppf(float x) {
  return __builtin_bit_cast(float, __builtin_amdgcn_mov_dpp(__builtin_bit_cast(int, x), CTRL, 0xf, 0xf, true));
}
constexpr int XOR1 = 0xB1, XOR2 = 0x4E, XOR7 = 0x141, ROR8 = 0x128, ROR4 = 0x124;
constexpr int QP_0101 = 0x44, QP_2323 = 0xEE, QP_0022 = 0xA0, QP_1133 = 0xF5;
__device__ __forceinline__ unsigned fbits(float x) { return __builtin_bit_cast(unsigned, x); }
__device__ __forceinline__ float bitsf(unsigned x) { return __builtin_bit_cast(float, x); }
__device__ __forceinline__ unsigned cvtpk(float lo, float hi) {
  bf16x2_t r = {(__bf16)lo, (__bf16)hi};
  return __builtin_bit_cast(unsigned, r);
}
__device__ __forceinline__ float tr16(float a, float b) {
  auto r = __builtin_amdgcn_permlane16_swap(fbits(a), fbits(b), false, false);
  return bitsf(r[0]) + bitsf(r[1]);
}
__device__ __forceinline__ float tr32(float a, float b) {
  auto r = __builtin_amdgcn_permlane32_swap(fbits(a), fbits(b), false, false);
  return bitsf(r[0]) + bitsf(r[1]);
}
template <int CTRL> __device__ __forceinline__ float trd(float a, float b, bool hi) {
  const float t1 = a + dppf<CTRL>(a), t2 = b + dppf<CTRL>(b);
  return hi ? t2 : t1;
}
__device__ __forceinline__ float max16(float x) {
  auto r = __builtin_amdgcn_permlane16_swap(fbits(x), fbits(x), false, false);
  return fmaxf(bitsf(r[0]), bitsf(r[1]));
}
__device__ __forceinline__ float max32(float x) {
  auto r = __builtin_amdgcn_permlane32_swap(fbits(x), fbits(x), false, false);
  return fmaxf(bitsf(r[0]), bitsf(r[1]));
}
__device__ __forceinline__ float wave_sum(float x) {
  x += dppf<XOR1>(x); x += dppf<XOR2>(x); x += dppf<ROR4>(x); x += dppf<ROR8>(x);
  x = tr16(x, x); return tr32(x, x);
}
__device__ __forceinline__ unsigned nosent(float x) { const unsigned u = fbits(x); return u == SENT ? 0x7FC00000u : u; }
__device__ __forceinline__ float ex2(float x) { return __builtin_amdgcn_exp2f(x); }
__device__ __forceinline__ float bflo(unsigned u) { return bitsf(u << 16); }
__device__ __forceinline__ float bfhi(unsigned u) { return bitsf(u & 0xffff0000u); }
__device__ __forceinline__ unsigned perm_lo(unsigned a, unsigned b) { return __builtin_amdgcn_perm(b, a, 0x05040100u); }
__device__ __forceinline__ unsigned perm_hi(unsigned a, unsigned b) { return __builtin_amdgcn_perm(b, a, 0x07060302u); }

#define RSRC(p, n) __builtin_amdgcn_make_buffer_rsrc((void*)(p), 0, (int)(n), 0x00020000)
#define LD128(r, vo, so, aux) __builtin_amdgcn_raw_buffer_load_b128(r, vo, so, aux)
#define LD64(r, vo, so, aux) __builtin_amdgcn_raw_buffer_load_b64(r, vo, so, aux)
#define LD32(r, vo, so, aux) __builtin_amdgcn_raw_buffer_load_b32(r, vo, so, aux)
#define ST128(v, r, vo, so, aux) __builtin_amdgcn_raw_buffer_store_b128(v, r, vo, so, aux)
#define ST64(v, r, vo, so, aux) __builtin_amdgcn_raw_buffer_store_b64(v, r, vo, so, aux)
constexpr int AUX_NT = 2, AUX_SC1 = 16, AUX_POLL = 16;
#define FENCE() asm volatile("" ::: "memory")
#define BARRIER() do { asm volatile("s_waitcnt lgkmcnt(0)" ::: "memory"); __builtin_amdgcn_s_barrier(); asm volatile("" ::: "memory"); } while (0)
#define DRAIN() asm volatile("s_waitcnt vmcnt(0)" ::: "memory")

constexpr int L_Q = 0;
constexpr int L_PP = 1024;
constexpr int L_PART = L_PP + NSW * 128;
constexpr int L_STAT = L_PART + NSW * 2048;
constexpr int L_END = L_STAT + NSW * 16;

#define STAMP(i) do {} while (0)
}

__global__ void __launch_bounds__(NTHR) gm_fused(const unsigned short* __restrict__ x, const unsigned short* __restrict__ kc,
                                                 const unsigned short* __restrict__ vc, const unsigned short* __restrict__ wq,
                                                 const unsigned short* __restrict__ wo, const unsigned short* __restrict__ w1,
                                                 const unsigned short* __restrict__ b1, const unsigned short* __restrict__ w2,
                                                 const unsigned short* __restrict__ b2, const unsigned short* __restrict__ gw_,
                                                 unsigned short* __restrict__ out, unsigned char* ws) {
  __shared__ __attribute__((aligned(16))) unsigned char lds[L_END];
  const int wg = blockIdx.x;
  const int wave = (__builtin_amdgcn_readfirstlane(threadIdx.x >> 6) + NSW) % (NSW + 1), lane = threadIdx.x & 63;
  const int head = (wg >> 3) & 7;
  const int rank = 4 * (wg & 7) + (wg >> 6);
  const __amdgpu_buffer_rsrc_t rws = RSRC(ws, WS_END);
  STAMP(0);

  if (wave < NSW) {
    const int hi = lane >> 5, c = lane & 31;
    const __amdgpu_buffer_rsrc_t rk = RSRC(kc + (size_t)head * SK * DH, HEAD_BYTES);
    const __amdgpu_buffer_rsrc_t rv = RSRC(vc + (size_t)head * SK * DH, HEAD_BYTES);
    const int gwv = rank * NSW + wave;
    const int voff = lane * 16;
    u32x4 kb[T], vb[T];
    unsigned so = (unsigned)gwv * (T * 1024);
    BARRIER();
#pragma unroll
    for (int t = 0; t < T; ++t) kb[t] = LD128(rk, voff + (t & 3) * 1024, so + (t & ~3) * 1024, AUX_NT);
    FENCE();
#pragma unroll
    for (int t = 0; t < T; ++t) vb[t] = LD128(rv, voff + (t & 3) * 1024, so + (t & ~3) * 1024, AUX_NT);
    FENCE();
    BARRIER();
    STAMP(1);
    const u32x4 q0 = *(const u32x4*)(lds + L_Q + c * 16), q1 = *(const u32x4*)(lds + L_Q + 512 + c * 16);
    float m = -INFINITY, l = 0.f;
    float a0[8], a1[8];
#pragma unroll
    for (int k = 0; k < 8; ++k) { a0[k] = 0.f; a1[k] = 0.f; }
    unsigned char* pp = lds + L_PP + wave * 128 + hi * 64;
    const int ppw = ((c & 1) * 8 + (c >> 2)) * 4;
    const bool b3 = c & 8, b2_ = c & 4, b1_ = c & 2, b0 = c & 1;
#pragma unroll 1
    for (int step = 0; step < NSTEP; ++step) {
      so += GW * T * 1024;
      float xs[2 * T];
#pragma unroll
      for (int t = 0; t < T; ++t) {
        float d0 = dot2(kb[t][0], q0[0], 0.f), d1 = dot2(kb[t][0], q1[0], 0.f);
#pragma unroll
        for (int j = 1; j < 4; ++j) { d0 = dot2(kb[t][j], q0[j], d0); d1 = dot2(kb[t][j], q1[j], d1); }
        asm volatile("" : "+v"(d0), "+v"(d1) :: "memory");
        xs[2 * t] = d0; xs[2 * t + 1] = d1;
        kb[t] = LD128(rk, voff + (t & 3) * 1024, so + (t & ~3) * 1024, AUX_NT);
        FENCE();
      }
#pragma unroll
      for (int i = 0; i < 16; ++i) xs[i] = tr16(xs[i], xs[i + 16]);
#pragma unroll
      for (int i = 0; i < 8; ++i) xs[i] = trd<ROR8>(xs[i], xs[i + 8], b3);
#pragma unroll
      for (int i = 0; i < 4; ++i) xs[i] = trd<XOR7>(xs[i], xs[i + 4], b2_);
#pragma unroll
      for (int i = 0; i < 2; ++i) xs[i] = trd<XOR2>(xs[i], xs[i + 2], b1_);
      const float s2 = trd<XOR1>(xs[0], xs[1], b0) * C2;
      if (__any(s2 > m)) {
        float pm = fmaxf(s2, dppf<XOR2>(s2));
        pm = fmaxf(pm, dppf<ROR4>(pm)); pm = fmaxf(pm, dppf<ROR8>(pm));
        pm = max32(max16(pm));
        const float mn = fmaxf(m, pm);
        const float al = ex2(m - mn);
        l *= al; m = mn;
        const float al0 = dppf<QP_0022>(al), al1 = dppf<QP_1133>(al);
#pragma unroll
        for (int k = 0; k < 8; ++k) { a0[k] *= al0; a1[k] *= al1; }
      }
      const float p = ex2(s2 - m);
      l += p;
      *(unsigned*)(pp + ppw) = cvtpk(dppf<QP_0101>(p), dppf<QP_2323>(p));
      FENCE();
      const u32x4 p0a = *(const u32x4*)(pp), p0b = *(const u32x4*)(pp + 16), p1a = *(const u32x4*)(pp + 32), p1b = *(const u32x4*)(pp + 48);
#pragma unroll
      for (int u = 0; u < T / 2; ++u) {
        unsigned lo[4], hh[4];
#pragma unroll
        for (int j = 0; j < 4; ++j) { lo[j] = perm_lo(vb[2 * u][j], vb[2 * u + 1][j]); hh[j] = perm_hi(vb[2 * u][j], vb[2 * u + 1][j]); }
        asm volatile("" : "+v"(lo[0]), "+v"(lo[1]), "+v"(lo[2]), "+v"(lo[3]), "+v"(hh[0]), "+v"(hh[1]), "+v"(hh[2]), "+v"(hh[3]) :: "memory");
        {
          const int t = 2 * u;
          vb[t] = LD128(rv, voff + (t & 3) * 1024, so + (t & ~3) * 1024, AUX_NT);
          vb[t + 1] = LD128(rv, voff + ((t + 1) & 3) * 1024, so + ((t + 1) & ~3) * 1024, AUX_NT);
        }
        FENCE();
        const unsigned w0 = u < 4 ? p0a[u & 3] : p0b[u & 3], w1_ = u < 4 ? p1a[u & 3] : p1b[u & 3];
#pragma unroll
        for (int j = 0; j < 4; ++j) {
          a0[2 * j] = dot2(lo[j], w0, a0[2 * j]); a0[2 * j + 1] = dot2(hh[j], w0, a0[2 * j + 1]);
          a1[2 * j] = dot2(lo[j], w1_, a1[2 * j]); a1[2 * j + 1] = dot2(hh[j], w1_, a1[2 * j + 1]);
        }
      }
    }
    STAMP(2);
    f32x4 r0, r1;
#pragma unroll
    for (int k = 0; k < 4; ++k) { r0[k] = tr32(a0[k], a1[k]); r1[k] = tr32(a0[k + 4], a1[k + 4]); }
    float* part = (float*)(lds + L_PART) + wave * 512 + hi * 256 + c * 8;
    *(f32x4*)part = r0; *(f32x4*)(part + 4) = r1;
    l += dppf<XOR2>(l); l += dppf<ROR4>(l); l += dppf<ROR8>(l); l = tr16(l, l); l = tr32(l, l);
    if (lane < 2) { float* st = (float*)(lds + L_STAT) + wave * 4 + lane * 2; st[0] = m; st[1] = l; }
    BARRIER();
    if (wave < 2) {
      const float* st = (const float*)(lds + L_STAT) + wave * 2;
      float mv[NSW], lv[NSW], M = -INFINITY;
#pragma unroll
      for (int v = 0; v < NSW; ++v) { mv[v] = st[4 * v]; lv[v] = st[4 * v + 1]; M = fmaxf(M, mv[v]); }
      f32x4 o = {0.f, 0.f, 0.f, 0.f}; float L = 0.f;
#pragma unroll
      for (int v = 0; v < NSW; ++v) {
        const float w = ex2(mv[v] - M);
        const f32x4 pv = *(const f32x4*)((const float*)(lds + L_PART) + v * 512 + wave * 256 + lane * 4);
        o += w * pv; L += w * lv[v];
      }
      const u32x4 ov = {nosent(o[0]), nosent(o[1]), nosent(o[2]), nosent(o[3])};
      ST128(ov, rws, WS_P1 + ((head * 32 + wave * 16 + (lane >> 2)) * 32 + rank) * 64 + (lane & 3) * 16, 0, AUX_SC1);
      if (lane == 0) { const u32x2 sv = {nosent(M), nosent(L)}; ST64(sv, rws, WS_S1 + (head * 2 + wave) * 256 + (rank & 15) * 16 + (rank >> 4) * 8, 0, AUX_SC1); }
      STAMP(3);
    }
    return;
  }

  const __amdgpu_buffer_rsrc_t rx = RSRC(x, DM * 2);
  const __amdgpu_buffer_rsrc_t rwq = RSRC(wq, DM * QW * 2);
  bool ok = true;
  u64 epoch;
  {
    const int half = lane & 1, rp = lane >> 1;
    const int colb = (head * 512 + rank * 16 + half * 8) * 2;
    u32x4 wa[7], wb[7]; unsigned xv[7];
#pragma unroll
    for (int i = 0; i < 7; ++i) {
      const int r = 2 * (rp + 32 * i);
      wa[i] = LD128(rwq, r * (QW * 2) + colb, 0, AUX_NT);
      wb[i] = LD128(rwq, (r + 1) * (QW * 2) + colb, 0, AUX_NT);
      xv[i] = LD32(rx, r * 2, 0, 0);
    }
    const u32x2 ev = LD64(rws, WS_EPOCH, 0, AUX_POLL);
    FENCE();
    const u32x4 sent4 = {SENT, SENT, SENT, SENT}; const u32x2 sent2 = {SENT, SENT};
#pragma unroll
    for (int g = 0; g < 2; ++g) ST128(sent4, rws, WS_P1 + ((head * 32 + g * 16 + (lane >> 2)) * 32 + rank) * 64 + (lane & 3) * 16, 0, AUX_SC1);
    if (lane < 2) ST64(sent2, rws, WS_S1 + (head * 2 + lane) * 256 + (rank & 15) * 16 + (rank >> 4) * 8, 0, AUX_SC1);
    if (lane < 56) {
      ST128(sent4, rws, WS_P2 + ((2 * lane) * NWG + wg) * 16, 0, AUX_SC1);
      ST128(sent4, rws, WS_P2 + ((2 * lane + 1) * NWG + wg) * 16, 0, AUX_SC1);
    }
    BARRIER();
    DRAIN();
    STAMP(9);
    epoch = ((u64)__builtin_amdgcn_readfirstlane(ev[1]) << 32 | __builtin_amdgcn_readfirstlane(ev[0])) + 1;
    float qa[8];
#pragma unroll
    for (int k = 0; k < 8; ++k) qa[k] = 0.f;
#pragma unroll
    for (int i = 0; i < 7; ++i)
#pragma unroll
      for (int j = 0; j < 4; ++j) {
        qa[2 * j] = dot2(perm_lo(wa[i][j], wb[i][j]), xv[i], qa[2 * j]);
        qa[2 * j + 1] = dot2(perm_hi(wa[i][j], wb[i][j]), xv[i], qa[2 * j + 1]);
      }
#pragma unroll
    for (int k = 0; k < 4; ++k) qa[k] = tr32(qa[k], qa[k + 4]);
#pragma unroll
    for (int k = 0; k < 2; ++k) qa[k] = tr16(qa[k], qa[k + 2]);
    float t = trd<XOR2>(qa[0], qa[1], lane & 2);
    t += dppf<ROR4>(t); t += dppf<ROR8>(t);
    const int k = ((lane >> 5) << 2) | (((lane >> 4) & 1) << 1) | ((lane >> 1) & 1);
    const unsigned qb = cvtpk(t, t) & 0xffffu;
    const u32x2 gr = {qb | ((unsigned)epoch << 16), (unsigned)(epoch >> 16)};
    if ((lane & 12) == 0) ST64(gr, rws, WS_Q + (head * 512 + rank * 16 + half * 8 + k) * 8, 0, AUX_SC1);
  }
  STAMP(1);
  const unsigned tag_lo = (unsigned)epoch & 0xffffu, tag_hi = (unsigned)(epoch >> 16);
  {
    u32x4 g[4];
    unsigned spins = 0;
    for (;;) {
      bool good = true;
#pragma unroll
      for (int i = 0; i < 4; ++i) g[i] = LD128(rws, WS_Q + head * 4096 + lane * 16 + i * 1024, 0, AUX_POLL);
#pragma unroll
      for (int i = 0; i < 4; ++i) good = good & ((g[i][0] >> 16) == tag_lo) & (g[i][1] == tag_hi) & ((g[i][2] >> 16) == tag_lo) & (g[i][3] == tag_hi);
      if (__all(good)) break;
      if (++spins > SPIN_CAP) { ok = false; break; }
      __builtin_amdgcn_s_sleep(2);
      FENCE();
    }
#pragma unroll
    for (int i = 0; i < 4; ++i) *(unsigned*)(lds + L_Q + (lane + 64 * i) * 4) = (g[i][0] & 0xffffu) | (g[i][2] << 16);
  }
  STAMP(2);
  BARRIER();
  float gate;
  {
    const __amdgpu_buffer_rsrc_t rg = RSRC(gw_, DM * 2);
    float s = 0.f;
#pragma unroll
    for (int i = 0; i < 4; ++i) {
      const unsigned a = LD32(rx, (lane + 64 * i) * 4, 0, 0), b = LD32(rg, (lane + 64 * i) * 4, 0, 0);
      s = dot2(a, b, s);
    }
    s = wave_sum(s);
    gate = 1.f / (1.f + ex2(-s * 1.4426950408889634f));
  }
  float pb[8];
#pragma unroll
  for (int k = 0; k < 8; ++k) pb[k] = 0.f;
  if (wg < FF / 8) {
    const __amdgpu_buffer_rsrc_t rw1 = RSRC(w1, DM * FF * 2);
    const __amdgpu_buffer_rsrc_t rw2 = RSRC(w2, FF * DM * 2);
    const __amdgpu_buffer_rsrc_t rb1 = RSRC(b1, FF * 2);
    float h[8];
#pragma unroll
    for (int k = 0; k < 8; ++k) h[k] = 0.f;
#pragma unroll
    for (int i = 0; i < 4; ++i) {
      const int r = 2 * (lane + 64 * i);
      const u32x4 wa = LD128(rw1, r * (FF * 2) + wg * 16, 0, AUX_NT), wb = LD128(rw1, (r + 1) * (FF * 2) + wg * 16, 0, AUX_NT);
      const unsigned xv = LD32(rx, r * 2, 0, 0);
#pragma unroll
      for (int j = 0; j < 4; ++j) {
        h[2 * j] = dot2(perm_lo(wa[j], wb[j]), xv, h[2 * j]);
        h[2 * j + 1] = dot2(perm_hi(wa[j], wb[j]), xv, h[2 * j + 1]);
      }
    }
    const u32x4 bb = LD128(rb1, wg * 16, 0, 0);
    u32x4 w2r[8];
#pragma unroll
    for (int f = 0; f < 8; ++f) w2r[f] = LD128(rw2, (wg * 8 + f) * (DM * 2) + (lane < 56 ? lane : 55) * 16, 0, AUX_NT);
#pragma unroll
    for (int k = 0; k < 8; ++k) {
      float v = wave_sum(h[k]) + ((k & 1) ? bfhi(bb[k >> 1]) : bflo(bb[k >> 1]));
      const float u = 0.7978845608028654f * (v + 0.044715f * v * v * v);
      h[k] = v / (1.f + ex2(-2.f * 1.4426950408889634f * u));
    }
#pragma unroll
    for (int f = 0; f < 8; ++f)
#pragma unroll
      for (int j = 0; j < 4; ++j) { pb[2 * j] += h[f] * bflo(w2r[f][j]); pb[2 * j + 1] += h[f] * bfhi(w2r[f][j]); }
  }
  unsigned wl[8][4], wh[8][4];
  {
    const __amdgpu_buffer_rsrc_t rwo = RSRC(wo, QW * DM * 2);
#pragma unroll
    for (int u = 0; u < 8; ++u) {
      const int r = head * 512 + rank * 16 + 2 * u;
      const u32x4 a = LD128(rwo, r * (DM * 2) + (lane < 56 ? lane : 55) * 16, 0, AUX_NT), b = LD128(rwo, (r + 1) * (DM * 2) + (lane < 56 ? lane : 55) * 16, 0, AUX_NT);
#pragma unroll
      for (int j = 0; j < 4; ++j) { wl[u][j] = perm_lo(a[j], b[j]); wh[u][j] = perm_hi(a[j], b[j]); }
    }
  }
  if (wg < NRED) {
    unsigned spins = 0;
    for (;;) {
      bool good = true;
#pragma unroll
      for (int i = 0; i < 4; ++i) { const u32x2 g = LD64(rws, WS_Q + (lane + 64 * i) * 128, 0, AUX_POLL); good = good & ((g[0] >> 16) == tag_lo) & (g[1] == tag_hi); }
      if (__all(good)) break;
      if (++spins > SPIN_CAP) { ok = false; break; }
      __builtin_amdgcn_s_sleep(8);
      FENCE();
    }
  }
  float b2v = 0.f;
  if (wg < NRED) { const unsigned short bv = b2[wg * 4 + (lane & 3)]; b2v = bitsf((unsigned)bv << 16); }
  STAMP(3);
  BARRIER();
  STAMP(4);
  float po[8];
  {
    u32x4 va, vb2, st;
    unsigned spins = 0;
    for (;;) {
      va = LD128(rws, WS_P1 + (head * 32 + rank) * 2048 + lane * 16, 0, AUX_POLL);
      vb2 = LD128(rws, WS_P1 + (head * 32 + rank) * 2048 + 1024 + lane * 16, 0, AUX_POLL);
      st = LD128(rws, WS_S1 + (head * 2 + (rank >> 4)) * 256 + (lane >> 2) * 16, 0, AUX_POLL);
      bool good = true;
#pragma unroll
      for (int k = 0; k < 4; ++k) good = good & (va[k] != SENT) & (vb2[k] != SENT) & (st[k] != SENT);
      if (__all(good)) break;
      if (++spins > SPIN_CAP) { ok = false; break; }
      __builtin_amdgcn_s_sleep(4);
      FENCE();
    }
    STAMP(5);
    float M = fmaxf(bitsf(st[0]), bitsf(st[2]));
    M = fmaxf(M, dppf<ROR4>(M)); M = fmaxf(M, dppf<ROR8>(M)); M = max32(max16(M));
    const float wa = ex2(bitsf(st[0]) - M), wb = ex2(bitsf(st[2]) - M);
    float L = wa * bitsf(st[1]) + wb * bitsf(st[3]);
    L += dppf<ROR4>(L); L += dppf<ROR8>(L); L = tr16(L, L); L = tr32(L, L);
    float o[4];
#pragma unroll
    for (int k = 0; k < 4; ++k) o[k] = wa * bitsf(va[k]) + wb * bitsf(vb2[k]);
    const float inv = 1.f / L;
#pragma unroll
    for (int k = 0; k < 2; ++k) {
      float t = tr32(o[k], o[k + 2]);
      t += dppf<ROR4>(t); t += dppf<ROR8>(t); t = tr16(t, t);
      o[k] = t * inv;
    }
    const unsigned pr = cvtpk(o[0], o[1]);
#pragma unroll
    for (int k = 0; k < 8; ++k) po[k] = 0.f;
#pragma unroll
    for (int u = 0; u < 8; ++u) {
      const unsigned s_ = __builtin_amdgcn_readlane(pr, (u >> 1) | ((u & 1) << 5));
#pragma unroll
      for (int j = 0; j < 4; ++j) { po[2 * j] = dot2(wl[u][j], s_, po[2 * j]); po[2 * j + 1] = dot2(wh[u][j], s_, po[2 * j + 1]); }
    }
  }
  {
    const float g1 = 1.f - gate;
    u32x4 s0, s1;
#pragma unroll
    for (int k = 0; k < 4; ++k) { s0[k] = nosent(gate * po[k] + g1 * pb[k]); s1[k] = nosent(gate * po[k + 4] + g1 * pb[k + 4]); }
    if (lane < 56) {
      ST128(s0, rws, WS_P2 + ((2 * lane) * NWG + wg) * 16, 0, AUX_SC1);
      ST128(s1, rws, WS_P2 + ((2 * lane + 1) * NWG + wg) * 16, 0, AUX_SC1);
    }
  }
  STAMP(6);
  if (wg >= NRED) return;
  {
    u32x4 v[4];
    unsigned spins = 0;
    for (;;) {
      bool good = true;
#pragma unroll
      for (int i = 0; i < 4; ++i) v[i] = LD128(rws, WS_P2 + wg * (NWG * 16) + i * 1024 + lane * 16, 0, AUX_POLL);
#pragma unroll
      for (int i = 0; i < 4; ++i) good = good & (v[i][0] != SENT) & (v[i][1] != SENT) & (v[i][2] != SENT) & (v[i][3] != SENT);
      if (__all(good)) break;
      if (++spins > SPIN_CAP) { ok = false; break; }
      __builtin_amdgcn_s_sleep(1);
      FENCE();
    }
    STAMP(7);
    float s[4];
#pragma unroll
    for (int k = 0; k < 4; ++k) s[k] = (bitsf(v[0][k]) + bitsf(v[1][k])) + (bitsf(v[2][k]) + bitsf(v[3][k]));
    s[0] = trd<XOR2>(s[0], s[2], lane & 2); s[1] = trd<XOR2>(s[1], s[3], lane & 2);
    float r = trd<XOR1>(s[0], s[1], lane & 1);
    r += dppf<ROR4>(r); r += dppf<ROR8>(r); r = tr16(r, r); r = tr32(r, r);
    r += (1.f - gate) * b2v;
    if (!ok) r = __builtin_nanf("");
    const unsigned lo = cvtpk(r, dppf<XOR1>(r));
    const unsigned hi2 = __builtin_amdgcn_mov_dpp((int)lo, XOR2, 0xf, 0xf, true);
    if (lane == 0) {
      const u32x2 ov = {lo, hi2};
      *(u32x2*)(out + wg * 4) = ov;
      if (wg == 0) { const u32x2 e = {(unsigned)epoch, (unsigned)(epoch >> 32)}; ST64(e, rws, WS_EPOCH, 0, AUX_SC1); }
    }
    STAMP(8);
  }
}

extern "C" void kernel_launch(void* const* d_in, const int* in_sizes, int n_in, void* d_out, int out_size, void* d_ws, size_t ws_size,
                              hipStream_t stream) {
  (void)in_sizes; (void)n_in; (void)out_size; (void)ws_size;
  hipLaunchKernelGGL(gm_fused, dim3(NWG), dim3(NTHR), 0, stream, (const unsigned short*)d_in[0], (const unsigned short*)d_in[1],
                     (const unsigned short*)d_in[2], (const unsigned short*)d_in[3], (const unsigned short*)d_in[4],
                     (const unsigned short*)d_in[6], (const unsigned short*)d_in[7], (const unsigned short*)d_in[8],
                     (const unsigned short*)d_in[9], (const unsigned short*)d_in[10], (unsigned short*)d_out, (unsigned char*)d_ws);
}
```

```cpp
#include <hip/hip_runtime.h>
#include <hip/hip_bf16.h>
#include <stdint.h>

typedef unsigned u32x4 __attribute__((ext_vector_type(4)));
typedef unsigned u32x2 __attribute__((ext_vector_type(2)));
typedef float f32x4 __attribute__((ext_vector_type(4)));
typedef __bf16 bf16x2_t __attribute__((ext_vector_type(2)));
typedef unsigned long long u64;

#define CPRV 8
#define NTOK 3
#define BSLOT 4
#define BLEN 1
#define NOMATH 0
#define NOSENT 0
#define NOW1 0
#define NOB 0
#define NSWV 6
#define NMAIN 2
#define KDELAY 0
#define VDELAY 0
#define H1SLEEP 4
#define STAMPS 0

namespace {
constexpr int DM = 448, SK = 65536, DH = 256, FF = 1792, QW = 4096;
constexpr int NWG = 256, NSW = NSWV, NTHR = (NSW + 2) * 64;
constexpr int T = 16;
constexpr int NTILE = SK / (2 * T);
constexpr int NSTEP = 1 + (NTILE / 32 - NSW) / NMAIN;
static_assert((NTILE / 32 - NSW) % NMAIN == 0, "");
constexpr unsigned HEAD_BYTES = (unsigned)SK * DH * 2;
constexpr int CPR = CPRV, NRED = DM / CPR;
constexpr unsigned SENT = 0xFFFFFFFFu;
constexpr float LOG2E = 1.4426950408889634f;
constexpr float C2 = 0.0625f * LOG2E;
constexpr unsigned WS_EPOCH = 0;
constexpr unsigned WS_R = 2048;
constexpr unsigned WS_Q = 4096;
constexpr unsigned WS_S1 = WS_Q + 8 * 512 * 8;
constexpr unsigned WS_P1 = WS_S1 + 8 * 2 * 32 * 8;
constexpr unsigned WS_P2 = WS_P1 + 8 * 32 * 32 * 64;
constexpr unsigned WS_END = WS_P2 + NWG * DM * 4;
constexpr unsigned WS_STAMP = 4u << 20;
constexpr unsigned SPIN_CAP = 1u << 18;

__device__ __forceinline__ float dot2(unsigned a, unsigned b, float c) {
  return __builtin_amdgcn_fdot2_f32_bf16(__builtin_bit_cast(bf16x2_t, a), __builtin_bit_cast(bf16x2_t, b), c, false);
}
template <int CTRL> __device__ __forceinline__ float dppf(float x) {
  return __builtin_bit_cast(float, __builtin_amdgcn_mov_dpp(__builtin_bit_cast(int, x), CTRL, 0xf, 0xf, true));
}
constexpr int XOR1 = 0xB1, XOR2 = 0x4E, XOR7 = 0x141, ROR8 = 0x128, ROR4 = 0x124;
constexpr int QP_0101 = 0x44, QP_2323 = 0xEE, QP_0022 = 0xA0, QP_1133 = 0xF5;
__device__ __forceinline__ unsigned fbits(float x) { return __builtin_bit_cast(unsigned, x); }
__device__ __forceinline__ float bitsf(unsigned x) { return __builtin_bit_cast(float, x); }
__device__ __forceinline__ unsigned cvtpk(float lo, float hi) {
  bf16x2_t r = {(__bf16)lo, (__bf16)hi};
  return __builtin_bit_cast(unsigned, r);
}
__device__ __forceinline__ float tr16(float a, float b) {
  auto r = __builtin_amdgcn_permlane16_swap(fbits(a), fbits(b), false, false);
  return bitsf(r[0]) + bitsf(r[1]);
}
__device__ __forceinline__ float tr32(float a, float b) {
  auto r = __builtin_amdgcn_permlane32_swap(fbits(a), fbits(b), false, false);
  return bitsf(r[0]) + bitsf(r[1]);
}
template <int CTRL> __device__ __forceinline__ float trd(float a, float b, bool hi) {
  const float t1 = a + dppf<CTRL>(a), t2 = b + dppf<CTRL>(b);
  return hi ? t2 : t1;
}
__device__ __forceinline__ float max16(float x) {
  auto r = __builtin_amdgcn_permlane16_swap(fbits(x), fbits(x), false, false);
  return fmaxf(bitsf(r[0]), bitsf(r[1]));
}
__device__ __forceinline__ float max32(float x) {
  auto r = __builtin_amdgcn_permlane32_swap(fbits(x), fbits(x), false, false);
  return fmaxf(bitsf(r[0]), bitsf(r[1]));
}
__device__ __forceinline__ float wave_sum(float x) {
  x += dppf<XOR1>(x); x += dppf<XOR2>(x); x += dppf<ROR4>(x); x += dppf<ROR8>(x);
  x = tr16(x, x); return tr32(x, x);
}
__device__ __forceinline__ unsigned nosent(float x) { const unsigned u = fbits(x); return u == SENT ? 0x7FC00000u : u; }
__device__ __forceinline__ float ex2(float x) { return __builtin_amdgcn_exp2f(x); }
__device__ __forceinline__ float bflo(unsigned u) { return bitsf(u << 16); }
__device__ __forceinline__ float bfhi(unsigned u) { return bitsf(u & 0xffff0000u); }
__device__ __forceinline__ unsigned perm_lo(unsigned a, unsigned b) { return __builtin_amdgcn_perm(b, a, 0x05040100u); }
__device__ __forceinline__ unsigned perm_hi(unsigned a, unsigned b) { return __builtin_amdgcn_perm(b, a, 0x07060302u); }

#define RSRC(p, n) __builtin_amdgcn_make_buffer_rsrc((void*)(p), 0, (int)(n), 0x00020000)
#define LD128(r, vo, so, aux) __builtin_amdgcn_raw_buffer_load_b128(r, vo, so, aux)
#define LD64(r, vo, so, aux) __builtin_amdgcn_raw_buffer_load_b64(r, vo, so, aux)
#define LD32(r, vo, so, aux) __builtin_amdgcn_raw_buffer_load_b32(r, vo, so, aux)
#define ST128(v, r, vo, so, aux) __builtin_amdgcn_raw_buffer_store_b128(v, r, vo, so, aux)
#define ST64(v, r, vo, so, aux) __builtin_amdgcn_raw_buffer_store_b64(v, r, vo, so, aux)
constexpr int AUX_NT = 2, AUX_SC1 = 16;
__device__ __forceinline__ u32x4 LD2X64(__amdgpu_buffer_rsrc_t r, int vo, int so) {
  const u32x2 a = LD64(r, vo, so, AUX_NT), b = LD64(r, vo + 448, so, AUX_NT);
  return u32x4{a[0], a[1], b[0], b[1]};
}
#define FENCE() asm volatile("" ::: "memory")
#define BARRIER() do { asm volatile("s_waitcnt lgkmcnt(0)" ::: "memory"); __builtin_amdgcn_s_barrier(); asm volatile("" ::: "memory"); __builtin_amdgcn_sched_barrier(0); } while (0)
#define NOW() ((unsigned)__builtin_amdgcn_s_memrealtime())
#define QFLAG() __builtin_amdgcn_readfirstlane(*(volatile const unsigned*)(lds + L_FLAG))
#define WAIT_TURN(t) do { while ((t) >= (unsigned)__builtin_amdgcn_readfirstlane(*(volatile const unsigned*)(lds + L_LAND)) + NTOK) __builtin_amdgcn_s_sleep(1); } while (0)
#define LANDED(n) do { if (lane == 0) __hip_atomic_fetch_add((unsigned*)(lds + L_LAND), (unsigned)(n), __ATOMIC_RELAXED, __HIP_MEMORY_SCOPE_WORKGROUP); } while (0)
#define DRAIN() asm volatile("s_waitcnt vmcnt(0)" ::: "memory")

constexpr int L_Q = 0;
constexpr int L_PP = 1024;
constexpr int L_PART = L_PP + NSW * 128;
constexpr int L_STAT = L_PART + NSW * 2048;
constexpr int L_FLAG = L_STAT + NSW * 16;
constexpr int L_TKT = L_FLAG + 4;
constexpr int L_LAND = L_FLAG + 8;
constexpr int L_END = L_FLAG + 16;
constexpr unsigned NSTATIC = 2 * NSW + BLEN;

#define STAMP(i) do {} while (0)
#define STAMPV(i, v) do {} while (0)
}

__global__ void __launch_bounds__(NTHR) gm_fused(const unsigned short* __restrict__ x, const unsigned short* __restrict__ kc,
                                                 const unsigned short* __restrict__ vc, const unsigned short* __restrict__ wq,
                                                 const unsigned short* __restrict__ wo, const unsigned short* __restrict__ w1,
                                                 const unsigned short* __restrict__ b1, const unsigned short* __restrict__ w2,
                                                 const unsigned short* __restrict__ b2, const unsigned short* __restrict__ gw_,
                                                 unsigned short* __restrict__ out, unsigned char* ws) {
  __shared__ __attribute__((aligned(16))) unsigned char lds[L_END];
  const int wg = blockIdx.x;
  asm volatile("" :: "s"(x), "s"(kc), "s"(vc), "s"(wq), "s"(wo), "s"(w1), "s"(b1), "s"(w2), "s"(b2), "s"(gw_), "s"(out), "s"(ws));
  const int hw = __builtin_amdgcn_readfirstlane(threadIdx.x >> 6), lane = threadIdx.x & 63;
  const int head = (wg >> 3) & 7;
  const int rank = 4 * (wg & 7) + (wg >> 6);
  const __amdgpu_buffer_rsrc_t rws = RSRC(ws, WS_END);
  const __amdgpu_buffer_rsrc_t rx = RSRC(x, DM * 2);
  STAMP(0);

  if (hw == 0) {
    const __amdgpu_buffer_rsrc_t rwq = RSRC(wq, DM * QW * 2);
    const int half = lane & 1, rp = lane >> 1;
    const int colb = (head * 512 + rank * 16 + half * 8) * 2;
    u32x4 wa[7], wb[7]; unsigned xv[7];
#pragma unroll
    for (int i = 0; i < 7; ++i) {
      wa[i] = LD128(rwq, rp * (QW * 4) + colb, i * (64 * QW * 2), AUX_NT);
      wb[i] = LD128(rwq, rp * (QW * 4) + colb, i * (64 * QW * 2) + QW * 2, AUX_NT);
    }
#pragma unroll
    for (int i = 0; i < 7; ++i) xv[i] = LD32(rx, rp * 4 + i * 128, 0, 0);
    u32x2 ev = LD64(rws, WS_EPOCH, 0, AUX_SC1);
    if (lane == 0) { const u32x4 z = {0u, NSTATIC, 0u, 0u}; *(u32x4*)(lds + L_FLAG) = z; }
    BARRIER();
#pragma unroll
    for (int i = 0; i < 7; ++i) asm volatile("" : "+v"(wa[i]), "+v"(wb[i]), "+v"(xv[i]));
    asm volatile("" : "+v"(ev));
    STAMP(9);
    const u64 epoch = ((u64)__builtin_amdgcn_readfirstlane(ev[1]) << 32 | __builtin_amdgcn_readfirstlane(ev[0])) + 1;
    float qa[8];
#pragma unroll
    for (int k = 0; k < 8; ++k) qa[k] = 0.f;
#pragma unroll
    for (int i = 0; i < 7; ++i)
#pragma unroll
      for (int j = 0; j < 4; ++j) {
        qa[2 * j] = dot2(perm_lo(wa[i][j], wb[i][j]), xv[i], qa[2 * j]);
        qa[2 * j + 1] = dot2(perm_hi(wa[i][j], wb[i][j]), xv[i], qa[2 * j + 1]);
      }
#pragma unroll
    for (int k = 0; k < 4; ++k) qa[k] = tr32(qa[k], qa[k + 4]);
#pragma unroll
    for (int k = 0; k < 2; ++k) qa[k] = tr16(qa[k], qa[k + 2]);
    float t = trd<XOR2>(qa[0], qa[1], lane & 2);
    t += dppf<ROR4>(t); t += dppf<ROR8>(t);
    const int k = ((lane >> 5) << 2) | (((lane >> 4) & 1) << 1) | ((lane >> 1) & 1);
    const unsigned qb = cvtpk(t, t) & 0xffffu;
    const u32x2 gr = {qb | ((unsigned)epoch << 16), (unsigned)(epoch >> 16)};
    if ((lane & 12) == 0) ST64(gr, rws, WS_Q + (head * 512 + rank * 16 + half * 8 + k) * 8, 0, AUX_SC1);
    FENCE();
    STAMP(1);
    const unsigned tag_lo = (unsigned)epoch & 0xffffu, tag_hi = (unsigned)(epoch >> 16);
    u32x4 g[4];
    unsigned spins = 0, fail = 0;
    for (;;) {
      bool good = true;
#pragma unroll
      for (int i = 0; i < 4; ++i) g[i] = LD128(rws, WS_Q + head * 4096 + lane * 16 + i * 1024, 0, AUX_SC1);
#pragma unroll
      for (int i = 0; i < 4; ++i) good = good & ((g[i][0] >> 16) == tag_lo) & (g[i][1] == tag_hi) & ((g[i][2] >> 16) == tag_lo) & (g[i][3] == tag_hi);
      if (__all(good)) break;
      if (++spins > SPIN_CAP) { fail = 1; break; }
      __builtin_amdgcn_s_sleep(1);
      FENCE();
    }
#pragma unroll
    for (int i = 0; i < 4; ++i) *(unsigned*)(lds + L_Q + (lane + 64 * i) * 4) = (g[i][0] & 0xffffu) | (g[i][2] << 16);
    asm volatile("s_waitcnt lgkmcnt(0)" ::: "memory");
    if (lane == 0) *(volatile unsigned*)(lds + L_FLAG) = 1u + fail;
    STAMP(2);
    STAMPV(0, spins);
    return;
  }

  if (hw >= 2) {
    const int wave = hw - 2;
    const int hi = lane >> 5, c = lane & 31;
    const __amdgpu_buffer_rsrc_t rk = RSRC(kc + (size_t)head * SK * DH, HEAD_BYTES);
    const __amdgpu_buffer_rsrc_t rv = RSRC(vc + (size_t)head * SK * DH, HEAD_BYTES);
    const int voff = lane * 16;
    u32x4 kb[T], vb[T];
    unsigned so = (unsigned)(rank * NSW + wave) * (T * 1024);
#define LOADK() do { _Pragma("unroll") for (int t = 0; t < T; ++t) { kb[t] = LD128(rk, voff + (t & 3) * 1024, so + (t & ~3) * 1024, AUX_NT); FENCE(); } } while (0)
#define LOADV() do { _Pragma("unroll") for (int t = 0; t < T; ++t) { vb[t] = LD128(rv, voff + (t & 3) * 1024, so + (t & ~3) * 1024, AUX_NT); FENCE(); } } while (0)
    BARRIER();
    {
      const bool mainw = wave < NMAIN;
      unsigned sk = mainw ? wave : 2 * NMAIN + 2 * (wave - NMAIN), sv = mainw ? NMAIN + wave : sk + 1;
      sk += sk >= BSLOT ? BLEN : 0; sv += sv >= BSLOT ? BLEN : 0;
      WAIT_TURN(sk);
      LOADK();
      if (sv >= sk + NTOK) { DRAIN(); LANDED(1); }
      WAIT_TURN(sv);
      LOADV();
      if (sv < sk + NTOK) { asm volatile("s_waitcnt vmcnt(16)" ::: "memory"); LANDED(1); }
      DRAIN(); LANDED(1);
      while (!QFLAG()) __builtin_amdgcn_s_sleep(1);
      FENCE();
    }
    STAMP(1);
    const u32x4 q0 = *(const u32x4*)(lds + L_Q + c * 16), q1 = *(const u32x4*)(lds + L_Q + 512 + c * 16);
    float m = -INFINITY, l = 0.f;
    float a0[8], a1[8];
#pragma unroll
    for (int k = 0; k < 8; ++k) { a0[k] = 0.f; a1[k] = 0.f; }
    unsigned char* pp = lds + L_PP + wave * 128 + hi * 64;
    const int ppw = ((c & 1) * 8 + (c >> 2)) * 4;
    const bool b3 = c & 8, b2_ = c & 4, b1_ = c & 2, b0 = c & 1;
    const int nstep = wave < NMAIN ? NSTEP : 1;
    so = (unsigned)(32 * NSW + rank * NMAIN + wave) * (T * 1024);
    float xs[2 * T];
#define QK() do { \
      _Pragma("unroll") for (int t = 0; t < T; ++t) { \
        float d0 = dot2(kb[t][0], q0[0], 0.f), d1 = dot2(kb[t][0], q1[0], 0.f); \
        _Pragma("unroll") for (int j = 1; j < 4; ++j) { d0 = dot2(kb[t][j], q0[j], d0); d1 = dot2(kb[t][j], q1[j], d1); } \
        xs[2 * t] = d0; xs[2 * t + 1] = d1; \
      } \
      _Pragma("unroll") for (int i = 0; i < 2 * T; i += 8) \
        asm volatile("" : "+v"(xs[i]), "+v"(xs[i + 1]), "+v"(xs[i + 2]), "+v"(xs[i + 3]), "+v"(xs[i + 4]), "+v"(xs[i + 5]), "+v"(xs[i + 6]), "+v"(xs[i + 7]) :: "memory"); \
      __builtin_amdgcn_sched_barrier(0); } while (0)
    QK();
    unsigned tk = 0;
#pragma unroll 1
    for (int step = 1;; ++step) {
#pragma unroll
      for (int i = 0; i < 16; ++i) xs[i] = tr16(xs[i], xs[i + 16]);
#pragma unroll
      for (int i = 0; i < 8; ++i) xs[i] = trd<ROR8>(xs[i], xs[i + 8], b3);
#pragma unroll
      for (int i = 0; i < 4; ++i) xs[i] = trd<XOR7>(xs[i], xs[i + 4], b2_);
#pragma unroll
      for (int i = 0; i < 2; ++i) xs[i] = trd<XOR2>(xs[i], xs[i + 2], b1_);
      const float s2 = trd<XOR1>(xs[0], xs[1], b0) * C2;
      if (__any(s2 > m)) {
        float pm = fmaxf(s2, dppf<XOR2>(s2));
        pm = fmaxf(pm, dppf<ROR4>(pm)); pm = fmaxf(pm, dppf<ROR8>(pm));
        pm = max32(max16(pm));
        const float mn = fmaxf(m, pm);
        const float al = ex2(m - mn);
        l *= al; m = mn;
        const float al0 = dppf<QP_0022>(al), al1 = dppf<QP_1133>(al);
#pragma unroll
        for (int k = 0; k < 8; ++k) { a0[k] *= al0; a1[k] *= al1; }
      }
      const float p = ex2(s2 - m);
      l += p;
      *(unsigned*)(pp + ppw) = cvtpk(dppf<QP_0101>(p), dppf<QP_2323>(p));
      FENCE();
      const u32x4 p0a = *(const u32x4*)(pp), p0b = *(const u32x4*)(pp + 16), p1a = *(const u32x4*)(pp + 32), p1b = *(const u32x4*)(pp + 48);
#pragma unroll
      for (int u = 0; u < T / 2; ++u) {
        const unsigned w0 = u < 4 ? p0a[u & 3] : p0b[u & 3], w1_ = u < 4 ? p1a[u & 3] : p1b[u & 3];
#pragma unroll
        for (int j = 0; j < 4; ++j) {
          const unsigned lo = perm_lo(vb[2 * u][j], vb[2 * u + 1][j]), hh = perm_hi(vb[2 * u][j], vb[2 * u + 1][j]);
          a0[2 * j] = dot2(lo, w0, a0[2 * j]); a0[2 * j + 1] = dot2(hh, w0, a0[2 * j + 1]);
          a1[2 * j] = dot2(lo, w1_, a1[2 * j]); a1[2 * j + 1] = dot2(hh, w1_, a1[2 * j + 1]);
        }
      }
      asm volatile("" : "+v"(a0[0]), "+v"(a0[1]), "+v"(a0[2]), "+v"(a0[3]), "+v"(a0[4]), "+v"(a0[5]), "+v"(a0[6]), "+v"(a0[7]) :: "memory");
      asm volatile("" : "+v"(a1[0]), "+v"(a1[1]), "+v"(a1[2]), "+v"(a1[3]), "+v"(a1[4]), "+v"(a1[5]), "+v"(a1[6]), "+v"(a1[7]) :: "memory");
      __builtin_amdgcn_sched_barrier(0);
      if (step == nstep) break;
      if (KDELAY) __builtin_amdgcn_s_sleep(KDELAY);
      if (tk < NSTATIC + NTOK) {
        if (step > 1) LANDED(1);
        unsigned t_ = 0;
        if (lane == 0) t_ = __hip_atomic_fetch_add((unsigned*)(lds + L_TKT), 1u, __ATOMIC_RELAXED, __HIP_MEMORY_SCOPE_WORKGROUP);
        tk = __builtin_amdgcn_readfirstlane(t_);
        WAIT_TURN(tk);
      } else if (tk != ~0u) { LANDED(1); tk = ~0u; }
      LOADK();
      __builtin_amdgcn_sched_barrier(0);
      QK();
      if (VDELAY) __builtin_amdgcn_s_sleep(VDELAY);
      if (tk < NSTATIC + NTOK) {
        LANDED(1);
        unsigned t_ = 0;
        if (lane == 0) t_ = __hip_atomic_fetch_add((unsigned*)(lds + L_TKT), 1u, __ATOMIC_RELAXED, __HIP_MEMORY_SCOPE_WORKGROUP);
        tk = __builtin_amdgcn_readfirstlane(t_);
        WAIT_TURN(tk);
      } else if (tk != ~0u) { LANDED(1); tk = ~0u; }
      LOADV();
      so += 32 * NMAIN * T * 1024;
      __builtin_amdgcn_sched_barrier(0);
    }
    STAMP(2);
    f32x4 r0, r1;
#pragma unroll
    for (int k = 0; k < 4; ++k) { r0[k] = tr32(a0[k], a1[k]); r1[k] = tr32(a0[k + 4], a1[k + 4]); }
    float* part = (float*)(lds + L_PART) + wave * 512 + hi * 256 + c * 8;
    *(f32x4*)part = r0; *(f32x4*)(part + 4) = r1;
    l += dppf<XOR2>(l); l += dppf<ROR4>(l); l += dppf<ROR8>(l); l = tr16(l, l); l = tr32(l, l);
    if (lane < 2) { float* st = (float*)(lds + L_STAT) + wave * 4 + lane * 2; st[0] = m; st[1] = l; }
    BARRIER();
    if (wave < 2) {
      const float* st = (const float*)(lds + L_STAT) + wave * 2;
      float mv[NSW], lv[NSW], M = -INFINITY;
#pragma unroll
      for (int v = 0; v < NSW; ++v) { mv[v] = st[4 * v]; lv[v] = st[4 * v + 1]; M = fmaxf(M, mv[v]); }
      f32x4 o = {0.f, 0.f, 0.f, 0.f}; float L = 0.f;
#pragma unroll
      for (int v = 0; v < NSW; ++v) {
        const float w = ex2(mv[v] - M);
        const f32x4 pv = *(const f32x4*)((const float*)(lds + L_PART) + v * 512 + wave * 256 + lane * 4);
        o += w * pv; L += w * lv[v];
      }
      const u32x4 ov = {nosent(o[0]), nosent(o[1]), nosent(o[2]), nosent(o[3])};
      ST128(ov, rws, WS_P1 + ((head * 32 + wave * 16 + (lane >> 2)) * 32 + rank) * 64 + (lane & 3) * 16, 0, AUX_SC1);
      if (lane == 0) { const u32x2 sv = {nosent(M), nosent(L)}; ST64(sv, rws, WS_S1 + (head * 2 + wave) * 256 + (rank & 15) * 16 + (rank >> 4) * 8, 0, AUX_SC1); }
      STAMP(3);
    }
    return;
  }

  bool ok = true;
  const int l56 = lane < 56 ? lane : 55;
  const int fs = (wg & 7) * 28 + (wg >> 3);
  const bool has_mlp = (wg >> 3) < 28;
  const int p2a = WS_P2 + ((4 * l56) / CPR * NWG + wg) * (CPR * 4) + (4 * l56) % CPR * 4;
  const int p2b = WS_P2 + ((224 + 4 * l56) / CPR * NWG + wg) * (CPR * 4) + (224 + 4 * l56) % CPR * 4;
  float gate, b2v;
  float pb[8];
  unsigned wl[8][4], wh[8][4];
  u64 epoch;
  {
    BARRIER();
    WAIT_TURN(BSLOT + BLEN - 1);
    const u32x4 sent4 = {SENT, SENT, SENT, SENT}; const u32x2 sent2 = {SENT, SENT};
#pragma unroll
    for (int g = 0; g < 2; ++g) ST128(sent4, rws, WS_P1 + ((head * 32 + g * 16 + (lane >> 2)) * 32 + rank) * 64 + (lane & 3) * 16, 0, AUX_SC1);
    if (lane < 2) ST64(sent2, rws, WS_S1 + (head * 2 + lane) * 256 + (rank & 15) * 16 + (rank >> 4) * 8, 0, AUX_SC1);
    ST128(sent4, rws, p2a, 0, AUX_SC1);
    ST128(sent4, rws, p2b, 0, AUX_SC1);
    FENCE();
    const __amdgpu_buffer_rsrc_t rg = RSRC(gw_, DM * 2);
    const __amdgpu_buffer_rsrc_t rw1 = RSRC(w1, has_mlp && !NOB && !NOW1 ? DM * FF * 2 : 0);
    const __amdgpu_buffer_rsrc_t rw2 = RSRC(w2, has_mlp && !NOB ? FF * DM * 2 : 0);
    const __amdgpu_buffer_rsrc_t rb1 = RSRC(b1, has_mlp ? FF * 2 : 0);
    const __amdgpu_buffer_rsrc_t rb2 = RSRC(b2, DM * 2);
    const __amdgpu_buffer_rsrc_t rwo = RSRC(wo, NOB ? 0 : QW * DM * 2);
    u32x2 ev = LD64(rws, WS_EPOCH, 0, AUX_SC1);
    u32x4 wa[4], wb[4], w2r[8], woa[8], wob[8], bb; unsigned xv[4], gv[4], b2d;
#pragma unroll
    for (int i = 0; i < 4; ++i) {
      wa[i] = LD128(rw1, lane * (FF * 4) + fs * 16, i * (128 * FF * 2), AUX_NT);
      wb[i] = LD128(rw1, lane * (FF * 4) + fs * 16, i * (128 * FF * 2) + FF * 2, AUX_NT);
      xv[i] = LD32(rx, lane * 4 + i * 256, 0, 0);
      gv[i] = LD32(rg, lane * 4 + i * 256, 0, 0);
    }
    bb = LD128(rb1, fs * 16, 0, 0);
    b2d = LD32(rb2, (wg * CPR + 4 * (lane & (CPR / 4 - 1)) + 2 * (lane >> 5)) * 2, 0, 0);
#pragma unroll
    for (int f = 0; f < 8; ++f) w2r[f] = LD2X64(rw2, l56 * 8, (fs * 8 + f) * (DM * 2));
#pragma unroll
    for (int u = 0; u < 8; ++u) {
      woa[u] = LD2X64(rwo, l56 * 8 + (head * 512 + rank * 16) * (DM * 2), (2 * u) * (DM * 2));
      wob[u] = LD2X64(rwo, l56 * 8 + (head * 512 + rank * 16) * (DM * 2), (2 * u + 1) * (DM * 2));
    }
    FENCE();
    DRAIN();
    LANDED(BLEN);
    asm volatile("" : "+v"(ev), "+v"(bb), "+v"(b2d));
    epoch = ((u64)__builtin_amdgcn_readfirstlane(ev[1]) << 32 | __builtin_amdgcn_readfirstlane(ev[0])) + 1;
    if (lane == 0) { const u32x2 gr = {(unsigned)epoch, (unsigned)(epoch >> 32)}; ST64(gr, rws, WS_R + wg * 8, 0, AUX_SC1); }
    STAMP(9);
#pragma unroll
    for (int i = 0; i < 4; ++i) asm volatile("" : "+v"(wa[i]), "+v"(wb[i]), "+v"(xv[i]), "+v"(gv[i]));
    float s = 0.f;
#pragma unroll
    for (int i = 0; i < 4; ++i) s = dot2(xv[i], gv[i], s);
    s = wave_sum(s);
    gate = 1.f / (1.f + ex2(-s * LOG2E));
    b2v = ((lane & 16) ? bfhi(b2d) : bflo(b2d)) * (1.f - gate);
    float h[8];
#pragma unroll
    for (int k = 0; k < 8; ++k) { h[k] = 0.f; pb[k] = 0.f; }
#pragma unroll
    for (int i = 0; i < 4; ++i)
#pragma unroll
      for (int j = 0; j < 4; ++j) {
        h[2 * j] = dot2(perm_lo(wa[i][j], wb[i][j]), xv[i], h[2 * j]);
        h[2 * j + 1] = dot2(perm_hi(wa[i][j], wb[i][j]), xv[i], h[2 * j + 1]);
      }
#pragma unroll
    for (int k = 0; k < 8; ++k) {
      const float v = wave_sum(h[k]) + ((k & 1) ? bfhi(bb[k >> 1]) : bflo(bb[k >> 1]));
      const float u = 0.7978845608028654f * (v + 0.044715f * v * v * v);
      h[k] = v / (1.f + ex2(-2.f * LOG2E * u));
    }
#pragma unroll
    for (int f = 0; f < 8; ++f)
#pragma unroll
      for (int j = 0; j < 4; ++j) { pb[2 * j] += h[f] * bflo(w2r[f][j]); pb[2 * j + 1] += h[f] * bfhi(w2r[f][j]); }
#pragma unroll
    for (int k = 0; k < 8; ++k) pb[k] *= 1.f - gate;
#pragma unroll
    for (int u = 0; u < 8; ++u)
#pragma unroll
      for (int j = 0; j < 4; ++j) { wl[u][j] = perm_lo(woa[u][j], wob[u][j]); wh[u][j] = perm_hi(woa[u][j], wob[u][j]); }
#pragma unroll
    for (int u = 0; u < 8; ++u)
      asm volatile("" : "+v"(wl[u][0]), "+v"(wl[u][1]), "+v"(wl[u][2]), "+v"(wl[u][3]), "+v"(wh[u][0]), "+v"(wh[u][1]), "+v"(wh[u][2]), "+v"(wh[u][3]), "+v"(pb[u]));
    asm volatile("" : "+v"(gate), "+v"(b2v));
  }
  {
    unsigned spins = 0;
    for (;;) {
      __builtin_amdgcn_s_sleep(64);
      bool good = true;
#pragma unroll
      for (int i = 0; i < 2; ++i) {
        const u32x4 g = LD128(rws, WS_R + lane * 16 + i * 1024, 0, AUX_SC1);
        good = good & (g[0] == (unsigned)epoch) & (g[1] == (unsigned)(epoch >> 32)) & (g[2] == (unsigned)epoch) & (g[3] == (unsigned)(epoch >> 32));
      }
      if (__all(good)) break;
      if (++spins > SPIN_CAP) { ok = false; break; }
      FENCE();
    }
  }
  STAMP(3);
  BARRIER();
  if (QFLAG() != 1u) ok = false;
  STAMP(4);
  float po[8];
  {
    u32x4 va, vb2, st;
    unsigned spins = 0;
    for (;;) {
      va = LD128(rws, WS_P1 + (head * 32 + rank) * 2048 + lane * 16, 0, AUX_SC1);
      vb2 = LD128(rws, WS_P1 + (head * 32 + rank) * 2048 + 1024 + lane * 16, 0, AUX_SC1);
      st = LD128(rws, WS_S1 + (head * 2 + (rank >> 4)) * 256 + (lane >> 2) * 16, 0, AUX_SC1);
      bool good = true;
#pragma unroll
      for (int k = 0; k < 4; ++k) good = good & (va[k] != SENT) & (vb2[k] != SENT) & (st[k] != SENT);
      if (__all(good)) break;
      if (++spins > SPIN_CAP) { ok = false; break; }
      __builtin_amdgcn_s_sleep(H1SLEEP);
      FENCE();
    }
    STAMP(5);
    float M = fmaxf(bitsf(st[0]), bitsf(st[2]));
    M = fmaxf(M, dppf<ROR4>(M)); M = fmaxf(M, dppf<ROR8>(M)); M = max32(max16(M));
    const float wa = ex2(bitsf(st[0]) - M), wb = ex2(bitsf(st[2]) - M);
    float L = wa * bitsf(st[1]) + wb * bitsf(st[3]);
    L += dppf<ROR4>(L); L += dppf<ROR8>(L); L = tr16(L, L); L = tr32(L, L);
    float o[4];
#pragma unroll
    for (int k = 0; k < 4; ++k) o[k] = wa * bitsf(va[k]) + wb * bitsf(vb2[k]);
    const float inv = 1.f / L;
#pragma unroll
    for (int k = 0; k < 2; ++k) {
      float t = tr32(o[k], o[k + 2]);
      t += dppf<ROR4>(t); t += dppf<ROR8>(t); t = tr16(t, t);
      o[k] = t * inv;
    }
    const unsigned pr = cvtpk(o[0], o[1]);
#pragma unroll
    for (int k = 0; k < 8; ++k) po[k] = 0.f;
#pragma unroll
    for (int u = 0; u < 8; ++u) {
      const unsigned s_ = __builtin_amdgcn_readlane(pr, (u >> 1) | ((u & 1) << 5));
#pragma unroll
      for (int j = 0; j < 4; ++j) { po[2 * j] = dot2(wl[u][j], s_, po[2 * j]); po[2 * j + 1] = dot2(wh[u][j], s_, po[2 * j + 1]); }
    }
  }
  {
    u32x4 s0, s1;
#pragma unroll
    for (int k = 0; k < 4; ++k) { s0[k] = nosent(gate * po[k] + pb[k]); s1[k] = nosent(gate * po[k + 4] + pb[k + 4]); }
    ST128(s0, rws, p2a, 0, AUX_SC1);
    ST128(s1, rws, p2b, 0, AUX_SC1);
  }
  STAMP(6);
  if (wg >= NRED) return;
  {
    constexpr int NL = CPR / 4 * 4;
    u32x4 v[NL];
    unsigned spins = 0;
    for (;;) {
      bool good = true;
#pragma unroll
      for (int i = 0; i < NL; ++i) v[i] = LD128(rws, WS_P2 + wg * (NWG * CPR * 4) + lane * 16 + (i & 3) * 1024, (i >> 2) * 4096, AUX_SC1);
#pragma unroll
      for (int i = 0; i < NL; ++i) good = good & (v[i][0] != SENT) & (v[i][1] != SENT) & (v[i][2] != SENT) & (v[i][3] != SENT);
      if (__all(good)) break;
      if (++spins > SPIN_CAP) { ok = false; break; }
      __builtin_amdgcn_s_sleep(1);
      FENCE();
    }
    STAMP(7);
    float s[4];
#pragma unroll
    for (int k = 0; k < 4; ++k) {
      float t[NL];
#pragma unroll
      for (int i = 0; i < NL; ++i) t[i] = bitsf(v[i][k]);
#pragma unroll
      for (int w = NL / 2; w >= 1; w >>= 1)
#pragma unroll
        for (int i = 0; i < w; ++i) t[i] += t[i + w];
      s[k] = t[0];
    }
    float r = tr16(tr32(s[0], s[2]), tr32(s[1], s[3]));
    r += dppf<ROR4>(r); r += dppf<ROR8>(r);
    if (CPR <= 8) r += dppf<XOR2>(r);
    if (CPR <= 4) r += dppf<XOR1>(r);
    r += b2v;
    if (!ok) r = __builtin_nanf("");
    auto sw = __builtin_amdgcn_permlane16_swap(fbits(r), fbits(r), false, false);
    const unsigned pk = cvtpk(bitsf(sw[0]), bitsf(sw[1]));
    if ((lane & 31) < CPR / 4) *(unsigned*)(out + wg * CPR + 4 * (lane & 31) + 2 * (lane >> 5)) = pk;
    if (lane == 0 && wg == 0) { const u32x2 e = {(unsigned)epoch, (unsigned)(epoch >> 32)}; ST64(e, rws, WS_EPOCH, 0, AUX_SC1); }
    STAMP(8);
  }
}

extern "C" void kernel_launch(void* const* d_in, const int* in_sizes, int n_in, void* d_out, int out_size, void* d_ws, size_t ws_size,
                              hipStream_t stream) {
  (void)in_sizes; (void)n_in; (void)out_size; (void)ws_size;
  hipLaunchKernelGGL(gm_fused, dim3(NWG), dim3(NTHR), 0, stream, (const unsigned short*)d_in[0], (const unsigned short*)d_in[1],
                     (const unsigned short*)d_in[2], (const unsigned short*)d_in[3], (const unsigned short*)d_in[4],
                     (const unsigned short*)d_in[6], (const unsigned short*)d_in[7], (const unsigned short*)d_in[8],
                     (const unsigned short*)d_in[9], (const unsigned short*)d_in[10], (unsigned short*)d_out, (unsigned char*)d_ws);
}
```

```cpp
#include <hip/hip_runtime.h>
#include <hip/hip_bf16.h>
#include <stdint.h>

typedef unsigned u32x4 __attribute__((ext_vector_type(4)));
typedef unsigned u32x2 __attribute__((ext_vector_type(2)));
typedef float f32x4 __attribute__((ext_vector_type(4)));
typedef __bf16 bf16x2_t __attribute__((ext_vector_type(2)));
typedef unsigned long long u64;
typedef __attribute__((address_space(3))) unsigned lds_u32;

namespace {
constexpr int DM = 448, SK = 65536, DH = 256, FF = 1792, QW = 4096;
constexpr int NWG = 256, NSW = 6, NMAIN = 2, NTHR = (NSW + 2) * 64;
constexpr int T = 16;
constexpr int NT = SK / (2 * T) / 32 / NMAIN;
constexpr unsigned TSTR = 32 * NMAIN * T * 1024;
static_assert(NMAIN == 2 && NSW == 6, "");
constexpr unsigned HEAD_BYTES = (unsigned)SK * DH * 2;
constexpr unsigned HSKEW = 3u << 20;
constexpr int NTOK = 3;
constexpr int BSLOT = 2;
constexpr unsigned NSTATIC = 2 * NSW + 1;
constexpr int CPR = 8, NRED = DM / CPR;
constexpr unsigned SENT = 0xFFFFFFFFu;
constexpr float LOG2E = 1.4426950408889634f;
constexpr float C2 = 0.0625f * LOG2E;
constexpr int OOB = 0x7ffffff0;
constexpr unsigned WS_EPOCH = 0;
constexpr unsigned WS_R = 2048;
constexpr unsigned WS_Q = 4096;
constexpr unsigned WS_S1 = WS_Q + 8 * 512 * 8;
constexpr unsigned WS_P1 = WS_S1 + 8 * 2 * 32 * 8;
constexpr unsigned WS_P2 = WS_P1 + 8 * 32 * 32 * 64;
constexpr unsigned WS_H1 = WS_P2 + NWG * DM * 4;
constexpr unsigned WS_END = WS_H1 + 224 * 256;
constexpr unsigned SPIN_CAP = 1u << 17;
constexpr int L_Q = 0;
constexpr int L_PP = 1024;
constexpr int L_PART = L_PP + NMAIN * 128;
constexpr int L_STAT = L_PART + NMAIN * 2048;
constexpr int L_S = L_STAT + NMAIN * 16;
constexpr int L_FLAG = L_S + NMAIN * NT * 256;
constexpr int L_TKT = L_FLAG + 4;
constexpr int L_LAND = L_FLAG + 8;
constexpr int L_HELP = L_FLAG + 12;
constexpr int L_END = L_FLAG + 16;

__device__ __forceinline__ float dot2(unsigned a, unsigned b, float c) {
  return __builtin_amdgcn_fdot2_f32_bf16(__builtin_bit_cast(bf16x2_t, a), __builtin_bit_cast(bf16x2_t, b), c, false);
}
template <int CTRL> __device__ __forceinline__ float dppf(float x) {
  return __builtin_bit_cast(float, __builtin_amdgcn_mov_dpp(__builtin_bit_cast(int, x), CTRL, 0xf, 0xf, true));
}
constexpr int XOR1 = 0xB1, XOR2 = 0x4E, XOR7 = 0x141, ROR8 = 0x128, ROR4 = 0x124;
constexpr int QP_0101 = 0x44, QP_2323 = 0xEE, QP_0022 = 0xA0, QP_1133 = 0xF5;
__device__ __forceinline__ unsigned fbits(float x) { return __builtin_bit_cast(unsigned, x); }
__device__ __forceinline__ float bitsf(unsigned x) { return __builtin_bit_cast(float, x); }
__device__ __forceinline__ unsigned cvtpk(float lo, float hi) {
  bf16x2_t r = {(__bf16)lo, (__bf16)hi};
  return __builtin_bit_cast(unsigned, r);
}
__device__ __forceinline__ float tr16(float a, float b) {
  auto r = __builtin_amdgcn_permlane16_swap(fbits(a), fbits(b), false, false);
  return bitsf(r[0]) + bitsf(r[1]);
}
__device__ __forceinline__ float tr32(float a, float b) {
  auto r = __builtin_amdgcn_permlane32_swap(fbits(a), fbits(b), false, false);
  return bitsf(r[0]) + bitsf(r[1]);
}
template <int CTRL> __device__ __forceinline__ float trd(float a, float b, bool hi) {
  const float t1 = a + dppf<CTRL>(a), t2 = b + dppf<CTRL>(b);
  return hi ? t2 : t1;
}
__device__ __forceinline__ float max16(float x) {
  auto r = __builtin_amdgcn_permlane16_swap(fbits(x), fbits(x), false, false);
  return fmaxf(bitsf(r[0]), bitsf(r[1]));
}
__device__ __forceinline__ float max32(float x) {
  auto r = __builtin_amdgcn_permlane32_swap(fbits(x), fbits(x), false, false);
  return fmaxf(bitsf(r[0]), bitsf(r[1]));
}
__device__ __forceinline__ float wave_sum(float x) {
  x += dppf<XOR1>(x); x += dppf<XOR2>(x); x += dppf<ROR4>(x); x += dppf<ROR8>(x);
  x = tr16(x, x); return tr32(x, x);
}
__device__ __forceinline__ unsigned nosent(float x) { return min(fbits(x), SENT - 1u); }
__device__ __forceinline__ float ex2(float x) { return __builtin_amdgcn_exp2f(x); }
__device__ __forceinline__ float bflo(unsigned u) { return bitsf(u << 16); }
__device__ __forceinline__ float bfhi(unsigned u) { return bitsf(u & 0xffff0000u); }
__device__ __forceinline__ unsigned perm_lo(unsigned a, unsigned b) { return __builtin_amdgcn_perm(b, a, 0x05040100u); }
__device__ __forceinline__ unsigned perm_hi(unsigned a, unsigned b) { return __builtin_amdgcn_perm(b, a, 0x07060302u); }

#define RSRC(p, n) __builtin_amdgcn_make_buffer_rsrc((void*)(p), 0, (int)(n), 0x00020000)
#define LD128(r, vo, so, aux) __builtin_amdgcn_raw_buffer_load_b128(r, vo, so, aux)
#define LD64(r, vo, so, aux) __builtin_amdgcn_raw_buffer_load_b64(r, vo, so, aux)
#define LD32(r, vo, so, aux) __builtin_amdgcn_raw_buffer_load_b32(r, vo, so, aux)
#define ST128(v, r, vo, so, aux) __builtin_amdgcn_raw_buffer_store_b128(v, r, vo, so, aux)
#define ST64(v, r, vo, so, aux) __builtin_amdgcn_raw_buffer_store_b64(v, r, vo, so, aux)
#define ST32(v, r, vo, so, aux) __builtin_amdgcn_raw_buffer_store_b32(v, r, vo, so, aux)
constexpr int AUX_NT = 2, AUX_SC1 = 16;
__device__ __forceinline__ u32x4 LD2X64(__amdgpu_buffer_rsrc_t r, int vo, int so) {
  const u32x2 a = LD64(r, vo, so, AUX_NT), b = LD64(r, vo + 448, so, AUX_NT);
  return u32x4{a[0], a[1], b[0], b[1]};
}
#define FENCE() asm volatile("" ::: "memory")
#define SCHED() __builtin_amdgcn_sched_barrier(0)
#define BARRIER() do { asm volatile("s_waitcnt lgkmcnt(0)" ::: "memory"); __builtin_amdgcn_s_barrier(); FENCE(); SCHED(); } while (0)
#define DRAIN() asm volatile("s_waitcnt vmcnt(0)" ::: "memory")
#define LDSW(off) ((lds_u32*)(lds + (off)))
#define LDS_LD(off) ((unsigned)__builtin_amdgcn_readfirstlane(__hip_atomic_load(LDSW(off), __ATOMIC_RELAXED, __HIP_MEMORY_SCOPE_WORKGROUP)))
#define WAIT_TURN(t) do { while ((t) >= LDS_LD(L_LAND) + NTOK) __builtin_amdgcn_s_sleep(1); } while (0)
#define LANDED() do { if (lane == 0) __hip_atomic_fetch_add(LDSW(L_LAND), 1u, __ATOMIC_RELAXED, __HIP_MEMORY_SCOPE_WORKGROUP); } while (0)

}

__global__ void __launch_bounds__(NTHR, 2) gm_fused(const unsigned short* __restrict__ x, const unsigned short* __restrict__ kc,
                                                 const unsigned short* __restrict__ vc, const unsigned short* __restrict__ wq,
                                                 const unsigned short* __restrict__ wo, const unsigned short* __restrict__ w1,
                                                 const unsigned short* __restrict__ b1, const unsigned short* __restrict__ w2,
                                                 const unsigned short* __restrict__ b2, const unsigned short* __restrict__ gw_,
                                                 unsigned short* __restrict__ out, unsigned char* ws) {
  __shared__ __attribute__((aligned(16))) unsigned char lds[L_END];
  const int wg = blockIdx.x;
  asm volatile("" :: "s"(x), "s"(kc), "s"(vc), "s"(wq), "s"(wo), "s"(w1), "s"(b1), "s"(w2), "s"(b2), "s"(gw_), "s"(out), "s"(ws));
  const int hw = __builtin_amdgcn_readfirstlane(threadIdx.x >> 6), lane = threadIdx.x & 63;
  const int head = (wg >> 3) & 7;
  const int rank = 4 * (wg & 7) + (wg >> 6);
  const __amdgpu_buffer_rsrc_t rws = RSRC(ws, WS_END);
  const __amdgpu_buffer_rsrc_t rx = RSRC(x, DM * 2);

  if (hw == 0) {
    const __amdgpu_buffer_rsrc_t rwq = RSRC(wq, DM * QW * 2);
    const int half = lane & 1, rp = lane >> 1;
    const int colb = (head * 512 + rank * 16 + half * 8) * 2;
    u32x4 wa[7], wb[7]; unsigned xv[7];
#pragma unroll
    for (int i = 0; i < 7; ++i) {
      wa[i] = LD128(rwq, rp * (QW * 4) + colb, i * (64 * QW * 2), AUX_NT);
      wb[i] = LD128(rwq, rp * (QW * 4) + colb, i * (64 * QW * 2) + QW * 2, AUX_NT);
    }
#pragma unroll
    for (int i = 0; i < 7; ++i) xv[i] = LD32(rx, rp * 4 + i * 128, 0, 0);
    u32x2 ev = LD64(rws, WS_EPOCH, 0, AUX_SC1);
    if (lane == 0) { const u32x4 z = {0u, NSTATIC, 0u, 0u}; *(u32x4*)(lds + L_FLAG) = z; }
    BARRIER();
#pragma unroll
    for (int i = 0; i < 7; ++i) asm volatile("" : "+v"(wa[i]), "+v"(wb[i]), "+v"(xv[i]));
    asm volatile("" : "+v"(ev));
    const u64 epoch = ((u64)__builtin_amdgcn_readfirstlane(ev[1]) << 32 | __builtin_amdgcn_readfirstlane(ev[0])) + 1;
    float qa[8];
#pragma unroll
    for (int k = 0; k < 8; ++k) qa[k] = 0.f;
#pragma unroll
    for (int i = 0; i < 7; ++i)
#pragma unroll
      for (int j = 0; j < 4; ++j) {
        qa[2 * j] = dot2(perm_lo(wa[i][j], wb[i][j]), xv[i], qa[2 * j]);
        qa[2 * j + 1] = dot2(perm_hi(wa[i][j], wb[i][j]), xv[i], qa[2 * j + 1]);
      }
#pragma unroll
    for (int k = 0; k < 4; ++k) qa[k] = tr32(qa[k], qa[k + 4]);
#pragma unroll
    for (int k = 0; k < 2; ++k) qa[k] = tr16(qa[k], qa[k + 2]);
    float t = trd<XOR2>(qa[0], qa[1], lane & 2);
    t += dppf<ROR4>(t); t += dppf<ROR8>(t);
    const int k = ((lane >> 5) << 2) | (((lane >> 4) & 1) << 1) | ((lane >> 1) & 1);
    const unsigned qb = cvtpk(t, t) & 0xffffu;
    const u32x2 gr = {qb | ((unsigned)epoch << 16), (unsigned)(epoch >> 16)};
    if ((lane & 12) == 0) ST64(gr, rws, WS_Q + (head * 512 + rank * 16 + half * 8 + k) * 8, 0, AUX_SC1);
    FENCE();
    const unsigned tag_lo = (unsigned)epoch & 0xffffu, tag_hi = (unsigned)(epoch >> 16);
    u32x4 g[4];
    unsigned spins = 0, fail = 0;
    for (;;) {
      bool good = true;
#pragma unroll
      for (int i = 0; i < 4; ++i) g[i] = LD128(rws, WS_Q + head * 4096 + lane * 16 + i * 1024, 0, AUX_SC1);
#pragma unroll
      for (int i = 0; i < 4; ++i) good = good & ((g[i][0] >> 16) == tag_lo) & (g[i][1] == tag_hi) & ((g[i][2] >> 16) == tag_lo) & (g[i][3] == tag_hi);
      if (__all(good)) break;
      if (++spins > SPIN_CAP) { fail = 1; break; }
      __builtin_amdgcn_s_sleep(1);
      FENCE();
    }
#pragma unroll
    for (int i = 0; i < 4; ++i) *(unsigned*)(lds + L_Q + (lane + 64 * i) * 4) = (g[i][0] & 0xffffu) | (g[i][2] << 16);
    asm volatile("s_waitcnt lgkmcnt(0)" ::: "memory");
    if (lane == 0) __hip_atomic_store(LDSW(L_FLAG), 1u + fail, __ATOMIC_RELAXED, __HIP_MEMORY_SCOPE_WORKGROUP);
    return;
  }

  if (hw >= 2) {
    const int wave = hw - 2, par = wave & 1;
    const bool mainw = wave < NMAIN;
    const int hi = lane >> 5, c = lane & 31;
    const __amdgpu_buffer_rsrc_t rk = RSRC(kc + (size_t)head * SK * DH, HEAD_BYTES);
    const __amdgpu_buffer_rsrc_t rv = RSRC(vc + (size_t)head * SK * DH, HEAD_BYTES);
    const int voff = lane * 16;
    u32x4 ka[T], kb[T];
    const unsigned so0 = (unsigned)(rank * NMAIN + par) * (T * 1024) + head * HSKEW;
#define LOADB(buf, r, n) do { const unsigned o_ = (so0 + (unsigned)(n) * TSTR) & (HEAD_BYTES - 1); \
      _Pragma("unroll") for (int t = 0; t < T; ++t) { buf[t] = LD128(r, voff + (t & 3) * 1024, o_ + (t & ~3) * 1024, AUX_NT); FENCE(); } SCHED(); } while (0)
    const int n0 = wave & ~1;
    {
      unsigned sk = mainw ? wave : 2 * NMAIN + 2 * (wave - NMAIN), sv = mainw ? NMAIN + wave : sk + 1;
      sk += sk >= BSLOT ? 1 : 0; sv += sv >= BSLOT ? 1 : 0;
      if (sk < NTOK) { LOADB(ka, rk, n0); BARRIER(); }
      else { BARRIER(); WAIT_TURN(sk); LOADB(ka, rk, n0); }
      if (sv >= sk + NTOK) { DRAIN(); LANDED(); }
      WAIT_TURN(sv);
      LOADB(kb, rk, n0 + 1);
      if (sv < sk + NTOK) { asm volatile("s_waitcnt vmcnt(16)" ::: "memory"); LANDED(); }
      DRAIN(); LANDED();
      while (!LDS_LD(L_FLAG)) __builtin_amdgcn_s_sleep(1);
      FENCE();
    }
    const u32x4 q0 = *(const u32x4*)(lds + L_Q + c * 16), q1 = *(const u32x4*)(lds + L_Q + 512 + c * 16);
    float* sc = (float*)(lds + L_S) + par * (NT * 64) + lane;
    const bool b3 = c & 8, b2_ = c & 4, b1_ = c & 2, b0 = c & 1;
    float m = -3.0e38f;
    float xs[2 * T];
    auto qk = [&](const u32x4 (&k)[T]) __attribute__((always_inline)) {
#pragma unroll
      for (int t = 0; t < T; ++t) {
        float d0 = dot2(k[t][0], q0[0], 0.f), d1 = dot2(k[t][0], q1[0], 0.f);
#pragma unroll
        for (int j = 1; j < 4; ++j) { d0 = dot2(k[t][j], q0[j], d0); d1 = dot2(k[t][j], q1[j], d1); }
        xs[2 * t] = d0; xs[2 * t + 1] = d1;
      }
#pragma unroll
      for (int i = 0; i < 2 * T; i += 8)
        asm volatile("" : "+v"(xs[i]), "+v"(xs[i + 1]), "+v"(xs[i + 2]), "+v"(xs[i + 3]), "+v"(xs[i + 4]), "+v"(xs[i + 5]), "+v"(xs[i + 6]), "+v"(xs[i + 7]) :: "memory");
      SCHED();
    };
    auto score = [&](int n) __attribute__((always_inline)) {
#pragma unroll
      for (int i = 0; i < 16; ++i) xs[i] = tr16(xs[i], xs[i + 16]);
#pragma unroll
      for (int i = 0; i < 8; ++i) xs[i] = trd<ROR8>(xs[i], xs[i + 8], b3);
#pragma unroll
      for (int i = 0; i < 4; ++i) xs[i] = trd<XOR7>(xs[i], xs[i + 4], b2_);
#pragma unroll
      for (int i = 0; i < 2; ++i) xs[i] = trd<XOR2>(xs[i], xs[i + 2], b1_);
      const float s2 = trd<XOR1>(xs[0], xs[1], b0) * C2;
      sc[n * 64] = s2;
      m = fmaxf(m, s2);
      asm volatile("" : "+v"(m) :: "memory");
      SCHED();
    };
    qk(ka);
    score(n0);
    qk(kb);
    if (!mainw) {
      score(n0 + 1);
      asm volatile("s_waitcnt lgkmcnt(0)" ::: "memory");
      if (lane == 0) __hip_atomic_fetch_add(LDSW(L_HELP), 1u << (8 * par), __ATOMIC_RELAXED, __HIP_MEMORY_SCOPE_WORKGROUP);
      return;
    }
    unsigned tk = 0;
    auto turn = [&](bool release) __attribute__((always_inline)) {
      if (tk < NSTATIC + NTOK) {
        if (release) LANDED();
        unsigned t_ = 0;
        if (lane == 0) t_ = __hip_atomic_fetch_add(LDSW(L_TKT), 1u, __ATOMIC_RELAXED, __HIP_MEMORY_SCOPE_WORKGROUP);
        tk = __builtin_amdgcn_readfirstlane(t_);
        WAIT_TURN(tk);
      } else if (tk != ~0u) { LANDED(); tk = ~0u; }
    };
    int np = 1;
#pragma unroll 1
    for (int n = NSW; n < NT; ++n) {
      turn(n > NSW);
      LOADB(kb, rk, n);
      score(np);
      qk(kb);
      np = n;
    }
    LOADB(kb, rv, 0);
    score(np);
    while (((LDS_LD(L_HELP) >> (8 * par)) & 0xffu) != 2u) __builtin_amdgcn_s_sleep(1);
    FENCE();
#pragma unroll
    for (int n = 2; n < NSW; ++n) m = fmaxf(m, sc[n * 64]);
    m = fmaxf(m, dppf<XOR2>(m)); m = fmaxf(m, dppf<ROR4>(m)); m = fmaxf(m, dppf<ROR8>(m));
    m = max32(max16(m));
    float l = 0.f;
    float a0[8], a1[8];
#pragma unroll
    for (int k = 0; k < 8; ++k) { a0[k] = 0.f; a1[k] = 0.f; }
    unsigned char* pp = lds + L_PP + wave * 128 + hi * 64;
    const int ppw = ((c & 1) * 8 + (c >> 2)) * 4;
#pragma unroll 1
    for (int n = 0;; ++n) {
      const float p = ex2(sc[n * 64] - m);
      l += p;
      *(unsigned*)(pp + ppw) = cvtpk(dppf<QP_0101>(p), dppf<QP_2323>(p));
      FENCE();
      u32x4 p0a = *(const u32x4*)(pp), p0b = *(const u32x4*)(pp + 16), p1a = *(const u32x4*)(pp + 32), p1b = *(const u32x4*)(pp + 48);
      asm volatile("" : "+v"(p0a), "+v"(p0b), "+v"(p1a), "+v"(p1b));
      SCHED();
#pragma unroll
      for (int u = 0; u < T / 2; ++u) {
        const unsigned w0 = u < 4 ? p0a[u & 3] : p0b[u & 3], w1_ = u < 4 ? p1a[u & 3] : p1b[u & 3];
#pragma unroll
        for (int j = 0; j < 4; ++j) {
          const unsigned lo = perm_lo(kb[2 * u][j], kb[2 * u + 1][j]), hh = perm_hi(kb[2 * u][j], kb[2 * u + 1][j]);
          a0[2 * j] = dot2(lo, w0, a0[2 * j]); a0[2 * j + 1] = dot2(hh, w0, a0[2 * j + 1]);
          a1[2 * j] = dot2(lo, w1_, a1[2 * j]); a1[2 * j + 1] = dot2(hh, w1_, a1[2 * j + 1]);
        }
      }
      asm volatile("" : "+v"(a0[0]), "+v"(a0[1]), "+v"(a0[2]), "+v"(a0[3]), "+v"(a0[4]), "+v"(a0[5]), "+v"(a0[6]), "+v"(a0[7]) :: "memory");
      asm volatile("" : "+v"(a1[0]), "+v"(a1[1]), "+v"(a1[2]), "+v"(a1[3]), "+v"(a1[4]), "+v"(a1[5]), "+v"(a1[6]), "+v"(a1[7]) :: "memory");
      SCHED();
      if (n == NT - 1) break;
      LOADB(kb, rv, n + 1);
    }
    f32x4 r0, r1;
#pragma unroll
    for (int k = 0; k < 4; ++k) { r0[k] = tr32(a0[k], a1[k]); r1[k] = tr32(a0[k + 4], a1[k + 4]); }
    float* part = (float*)(lds + L_PART) + wave * 512 + hi * 256 + c * 8;
    *(f32x4*)part = r0; *(f32x4*)(part + 4) = r1;
    l += dppf<XOR2>(l); l += dppf<ROR4>(l); l += dppf<ROR8>(l); l = tr16(l, l); l = tr32(l, l);
    if (lane < 2) { float* st = (float*)(lds + L_STAT) + wave * 4 + lane * 2; st[0] = m; st[1] = l; }
    BARRIER();
    {
      const float* st = (const float*)(lds + L_STAT) + wave * 2;
      float mv[NMAIN], lv[NMAIN], M = -3.0e38f;
#pragma unroll
      for (int v = 0; v < NMAIN; ++v) { mv[v] = st[4 * v]; lv[v] = st[4 * v + 1]; M = fmaxf(M, mv[v]); }
      f32x4 o = {0.f, 0.f, 0.f, 0.f}; float L = 0.f;
#pragma unroll
      for (int v = 0; v < NMAIN; ++v) {
        const float w = ex2(mv[v] - M);
        const f32x4 pv = *(const f32x4*)((const float*)(lds + L_PART) + v * 512 + wave * 256 + lane * 4);
        o += w * pv; L += w * lv[v];
      }
      const u32x4 ov = {nosent(o[0]), nosent(o[1]), nosent(o[2]), nosent(o[3])};
      ST128(ov, rws, WS_P1 + ((head * 32 + wave * 16 + (lane >> 2)) * 32 + rank) * 64 + (lane & 3) * 16, 0, AUX_SC1);
      if (lane == 0) { const u32x2 sv = {nosent(M), nosent(L)}; ST64(sv, rws, WS_S1 + (head * 2 + wave) * 256 + (rank & 15) * 16 + (rank >> 4) * 8, 0, AUX_SC1); }
    }
    return;
  }

  bool ok = true;
  const int l56 = lane < 56 ? lane : 55;
  const bool has_mlp = wg < FF / 8;
  const int h1o = has_mlp ? (int)WS_H1 + (((wg & ~7) + (lane & 7)) * 8 + (wg & 7)) * 32 + ((lane >> 5) * 4 + ((lane >> 4) & 1) * 2 + ((lane >> 3) & 1)) * 4 : OOB;
  const int p2a = WS_P2 + ((4 * l56) / CPR * NWG + wg) * (CPR * 4) + (4 * l56) % CPR * 4;
  const int p2b = WS_P2 + ((224 + 4 * l56) / CPR * NWG + wg) * (CPR * 4) + (224 + 4 * l56) % CPR * 4;
  float gate, b2v;
  float pb[8];
  u32x4 w2r[8]; unsigned b1d;
  unsigned wl[8][4], wh[8][4];
  u64 epoch;
  {
    const u32x4 sent4 = {SENT, SENT, SENT, SENT}; const u32x2 sent2 = {SENT, SENT};
#pragma unroll
    for (int g = 0; g < 2; ++g) ST128(sent4, rws, WS_P1 + ((head * 32 + g * 16 + (lane >> 2)) * 32 + rank) * 64 + (lane & 3) * 16, 0, AUX_SC1);
    if (lane < 2) ST64(sent2, rws, WS_S1 + (head * 2 + lane) * 256 + (rank & 15) * 16 + (rank >> 4) * 8, 0, AUX_SC1);
    ST128(sent4, rws, p2a, 0, AUX_SC1);
    ST128(sent4, rws, p2b, 0, AUX_SC1);
    ST32(SENT, rws, h1o, 0, AUX_SC1);
    BARRIER();
    WAIT_TURN(BSLOT);
    const __amdgpu_buffer_rsrc_t rg = RSRC(gw_, DM * 2);
    const __amdgpu_buffer_rsrc_t rw1 = RSRC(w1, has_mlp ? DM * FF * 2 : 0);
    const __amdgpu_buffer_rsrc_t rw2 = RSRC(w2, has_mlp ? FF * DM * 2 : 0);
    const __amdgpu_buffer_rsrc_t rb1 = RSRC(b1, has_mlp ? FF * 2 : 0);
    const __amdgpu_buffer_rsrc_t rb2 = RSRC(b2, DM * 2);
    const __amdgpu_buffer_rsrc_t rwo = RSRC(wo, QW * DM * 2);
    u32x2 ev = LD64(rws, WS_EPOCH, 0, AUX_SC1);
    u32x4 wa[4], wb[4], woa[8], wob[8]; unsigned xv[4], gv[4], xp[4], b2d;
#pragma unroll
    for (int i = 0; i < 4; ++i) {
      const bool in = i < 3 || lane < 32;
      const int vo = in ? ((wg & 7) * 56 + 2 * (lane >> 3)) * (FF * 2) + (wg >> 3) * 128 + (lane & 7) * 16 : OOB;
      wa[i] = LD128(rw1, vo, i * (16 * FF * 2), AUX_NT);
      wb[i] = LD128(rw1, vo, i * (16 * FF * 2) + FF * 2, AUX_NT);
      xp[i] = LD32(rx, in ? ((wg & 7) * 28 + (lane >> 3) + 8 * i) * 4 : OOB, 0, 0);
      xv[i] = LD32(rx, lane * 4 + i * 256, 0, 0);
      gv[i] = LD32(rg, lane * 4 + i * 256, 0, 0);
    }
    b1d = LD32(rb1, wg * 16 + (lane & 6) * 2, 0, 0);
    b2d = LD32(rb2, (wg * CPR + 4 * (lane & (CPR / 4 - 1)) + 2 * (lane >> 5)) * 2, 0, 0);
#pragma unroll
    for (int f = 0; f < 8; ++f) w2r[f] = LD2X64(rw2, l56 * 8, (wg * 8 + f) * (DM * 2));
#pragma unroll
    for (int u = 0; u < 8; ++u) {
      woa[u] = LD2X64(rwo, l56 * 8 + (head * 512 + rank * 16) * (DM * 2), (2 * u) * (DM * 2));
      wob[u] = LD2X64(rwo, l56 * 8 + (head * 512 + rank * 16) * (DM * 2), (2 * u + 1) * (DM * 2));
    }
    FENCE();
    DRAIN();
    LANDED();
    asm volatile("" : "+v"(ev), "+v"(b1d), "+v"(b2d));
    epoch = ((u64)__builtin_amdgcn_readfirstlane(ev[1]) << 32 | __builtin_amdgcn_readfirstlane(ev[0])) + 1;
    if (lane == 0) { const u32x2 gr = {(unsigned)epoch, (unsigned)(epoch >> 32)}; ST64(gr, rws, WS_R + wg * 8, 0, AUX_SC1); }
#pragma unroll
    for (int i = 0; i < 4; ++i) asm volatile("" : "+v"(wa[i]), "+v"(wb[i]), "+v"(xv[i]), "+v"(gv[i]), "+v"(xp[i]));
    float h[8];
#pragma unroll
    for (int k = 0; k < 8; ++k) h[k] = 0.f;
#pragma unroll
    for (int i = 0; i < 4; ++i)
#pragma unroll
      for (int j = 0; j < 4; ++j) {
        h[2 * j] = dot2(perm_lo(wa[i][j], wb[i][j]), xp[i], h[2 * j]);
        h[2 * j + 1] = dot2(perm_hi(wa[i][j], wb[i][j]), xp[i], h[2 * j + 1]);
      }
#pragma unroll
    for (int k = 0; k < 4; ++k) h[k] = tr32(h[k], h[k + 4]);
#pragma unroll
    for (int k = 0; k < 2; ++k) h[k] = tr16(h[k], h[k + 2]);
    ST32(nosent(trd<ROR8>(h[0], h[1], lane & 8)), rws, h1o, 0, AUX_SC1);
    float s = 0.f;
#pragma unroll
    for (int i = 0; i < 4; ++i) s = dot2(xv[i], gv[i], s);
    s = wave_sum(s);
    gate = 1.f / (1.f + ex2(-s * LOG2E));
    b2v = ((lane & 16) ? bfhi(b2d) : bflo(b2d)) * (1.f - gate);
#pragma unroll
    for (int u = 0; u < 8; ++u)
#pragma unroll
      for (int j = 0; j < 4; ++j) { wl[u][j] = perm_lo(woa[u][j], wob[u][j]); wh[u][j] = perm_hi(woa[u][j], wob[u][j]); }
#pragma unroll
    for (int u = 0; u < 8; ++u)
      asm volatile("" : "+v"(wl[u][0]), "+v"(wl[u][1]), "+v"(wl[u][2]), "+v"(wl[u][3]), "+v"(wh[u][0]), "+v"(wh[u][1]), "+v"(wh[u][2]), "+v"(wh[u][3]));
    asm volatile("" : "+v"(gate), "+v"(b2v));
  }
  {
    unsigned spins = 0;
    for (;;) {
      __builtin_amdgcn_s_sleep(64);
      bool good = true;
#pragma unroll
      for (int i = 0; i < 2; ++i) {
        const u32x4 g = LD128(rws, WS_R + lane * 16 + i * 1024, 0, AUX_SC1);
        good = good & (g[0] == (unsigned)epoch) & (g[1] == (unsigned)(epoch >> 32)) & (g[2] == (unsigned)epoch) & (g[3] == (unsigned)(epoch >> 32));
      }
      if (__all(good)) break;
      if (++spins > SPIN_CAP) { ok = false; break; }
      FENCE();
    }
  }
#pragma unroll
  for (int k = 0; k < 8; ++k) pb[k] = 0.f;
  if (has_mlp) {
    unsigned hv, spins = 0;
    for (;;) {
      hv = LD32(rws, WS_H1 + wg * 256 + lane * 4, 0, AUX_SC1);
      if (__all(hv != SENT)) break;
      if (++spins > SPIN_CAP) { ok = false; break; }
      __builtin_amdgcn_s_sleep(32);
      FENCE();
    }
    float v = bitsf(hv);
    v += dppf<ROR8>(v); v = tr16(v, v); v = tr32(v, v);
    v += (lane & 1) ? bfhi(b1d) : bflo(b1d);
    const float u = 0.7978845608028654f * (v + 0.044715f * v * v * v);
    const float hg = v / (1.f + ex2(-2.f * LOG2E * u)) * (1.f - gate);
#pragma unroll
    for (int f = 0; f < 8; ++f) {
      const float hf = bitsf(__builtin_amdgcn_readlane(fbits(hg), f));
#pragma unroll
      for (int j = 0; j < 4; ++j) { pb[2 * j] += hf * bflo(w2r[f][j]); pb[2 * j + 1] += hf * bfhi(w2r[f][j]); }
    }
  }
  asm volatile("" : "+v"(pb[0]), "+v"(pb[1]), "+v"(pb[2]), "+v"(pb[3]), "+v"(pb[4]), "+v"(pb[5]), "+v"(pb[6]), "+v"(pb[7]));
  BARRIER();
  if (LDS_LD(L_FLAG) != 1u) ok = false;
  {
    u32x4 va, vb2, st;
    unsigned spins = 0;
    for (;;) {
      va = LD128(rws, WS_P1 + (head * 32 + rank) * 2048 + lane * 16, 0, AUX_SC1);
      vb2 = LD128(rws, WS_P1 + (head * 32 + rank) * 2048 + 1024 + lane * 16, 0, AUX_SC1);
      st = LD128(rws, WS_S1 + (head * 2 + (rank >> 4)) * 256 + (lane >> 2) * 16, 0, AUX_SC1);
      bool good = true;
#pragma unroll
      for (int k = 0; k < 4; ++k) good = good & (va[k] != SENT) & (vb2[k] != SENT) & (st[k] != SENT);
      if (__all(good)) break;
      if (++spins > SPIN_CAP) { ok = false; break; }
      __builtin_amdgcn_s_sleep(1);
      FENCE();
    }
    float M = fmaxf(bitsf(st[0]), bitsf(st[2]));
    M = fmaxf(M, dppf<ROR4>(M)); M = fmaxf(M, dppf<ROR8>(M)); M = max32(max16(M));
    const float wa = ex2(bitsf(st[0]) - M), wb = ex2(bitsf(st[2]) - M);
    float L = wa * bitsf(st[1]) + wb * bitsf(st[3]);
    L += dppf<ROR4>(L); L += dppf<ROR8>(L); L = tr16(L, L); L = tr32(L, L);
    float o[4];
#pragma unroll
    for (int k = 0; k < 4; ++k) o[k] = wa * bitsf(va[k]) + wb * bitsf(vb2[k]);
    const float inv = __builtin_amdgcn_rcpf(L);
#pragma unroll
    for (int k = 0; k < 2; ++k) {
      float t = tr32(o[k], o[k + 2]);
      t += dppf<ROR4>(t); t += dppf<ROR8>(t); t = tr16(t, t);
      o[k] = t * inv;
    }
    const unsigned pr = cvtpk(o[0], o[1]);
    float po[8];
#pragma unroll
    for (int k = 0; k < 8; ++k) po[k] = 0.f;
#pragma unroll
    for (int u = 0; u < 8; ++u) {
      const unsigned s_ = __builtin_amdgcn_readlane(pr, (u >> 1) | ((u & 1) << 5));
#pragma unroll
      for (int j = 0; j < 4; ++j) { po[2 * j] = dot2(wl[u][j], s_, po[2 * j]); po[2 * j + 1] = dot2(wh[u][j], s_, po[2 * j + 1]); }
    }
    const float poison = ok ? 0.f : __builtin_nanf("");
    u32x4 s0, s1;
#pragma unroll
    for (int k = 0; k < 4; ++k) { s0[k] = nosent(gate * po[k] + (pb[k] + poison)); s1[k] = nosent(gate * po[k + 4] + (pb[k + 4] + poison)); }
    ST128(s0, rws, p2a, 0, AUX_SC1);
    ST128(s1, rws, p2b, 0, AUX_SC1);
  }
  if (wg >= NRED) return;
  {
    constexpr int NL = CPR;
    __builtin_amdgcn_s_sleep(12);
    u32x4 v[NL];
    unsigned spins = 0;
    for (;;) {
      bool good = true;
#pragma unroll
      for (int i = 0; i < NL; ++i) v[i] = LD128(rws, WS_P2 + wg * (NWG * CPR * 4) + lane * 16 + (i & 3) * 1024, (i >> 2) * 4096, AUX_SC1);
#pragma unroll
      for (int i = 0; i < NL; ++i) good = good & (v[i][0] != SENT) & (v[i][1] != SENT) & (v[i][2] != SENT) & (v[i][3] != SENT);
      if (__all(good)) break;
      if (++spins > SPIN_CAP) { ok = false; break; }
      FENCE();
    }
    float s[4];
#pragma unroll
    for (int k = 0; k < 4; ++k) {
      float t[NL];
#pragma unroll
      for (int i = 0; i < NL; ++i) t[i] = bitsf(v[i][k]);
#pragma unroll
      for (int w = NL / 2; w >= 1; w >>= 1)
#pragma unroll
        for (int i = 0; i < w; ++i) t[i] += t[i + w];
      s[k] = t[0];
    }
    float r = tr16(tr32(s[0], s[2]), tr32(s[1], s[3]));
    r += dppf<ROR4>(r); r += dppf<ROR8>(r); r += dppf<XOR2>(r);
    r += b2v;
    if (!ok) r = __builtin_nanf("");
    auto sw = __builtin_amdgcn_permlane16_swap(fbits(r), fbits(r), false, false);
    const unsigned pk = cvtpk(bitsf(sw[0]), bitsf(sw[1]));
    if ((lane & 31) < CPR / 4) *(unsigned*)(out + wg * CPR + 4 * (lane & 31) + 2 * (lane >> 5)) = pk;
    if (lane == 0 && wg == 0) { const u32x2 e = {(unsigned)epoch, (unsigned)(epoch >> 32)}; ST64(e, rws, WS_EPOCH, 0, AUX_SC1); }
  }
}

extern "C" void kernel_launch(void* const* d_in, const int* in_sizes, int n_in, void* d_out, int out_size, void* d_ws, size_t ws_size,
                              hipStream_t stream) {
  (void)in_sizes; (void)n_in; (void)out_size; (void)ws_size;
  hipLaunchKernelGGL(gm_fused, dim3(NWG), dim3(NTHR), 0, stream, (const unsigned short*)d_in[0], (const unsigned short*)d_in[1],
                     (const unsigned short*)d_in[2], (const unsigned short*)d_in[3], (const unsigned short*)d_in[4],
                     (const unsigned short*)d_in[6], (const unsigned short*)d_in[7], (const unsigned short*)d_in[8],
                     (const unsigned short*)d_in[9], (const unsigned short*)d_in[10], (unsigned short*)d_out, (unsigned char*)d_ws);
}
```
